```python
import torch

def _softmax(x, axis=-1):
    return torch.softmax(x, dim=axis)
def _gelu(x):
    return torch.nn.functional.gelu(x, approximate="tanh")
def _silu(x):
    return torch.nn.functional.silu(x)
def _sigmoid(x):
    return torch.sigmoid(x)
def _layernorm(x, axis=-1, eps=1e-5):
    m = x.mean(dim=axis, keepdim=True)
    v = x.var(dim=axis, keepdim=True, unbiased=False)
    return (x - m) / torch.sqrt(v + eps)
def _scatter_add(tgt, idx, src, axis=0):
    return tgt.clone().index_add_(axis, idx, src)
def _segment_sum(src, seg, n):
    out = torch.zeros((n,) + src.shape[1:], dtype=src.dtype)
    return out.index_add_(0, seg, src)
def _pad(x, pw):
    flat = tuple(v for pair in reversed(pw) for v in pair)
    return torch.nn.functional.pad(x, flat)

kernel_name = 'interleave_layers__spectral_gfnet__dense_activation_gelu__prompt'

def setup_inputs():

    _w = (lambda a: (lambda t: t.to(torch.bfloat16) if t.is_floating_point() else t)(torch.as_tensor(a)))
    def _A_setup():
        B, S, D = 1, 2048, 512
        Df = D // 2 + 1
        return {
            "x": _w((lambda *s: torch.randn(*s, dtype=torch.bfloat16) * 0.05)(B, S, D)),
            "w_r": _w((lambda *s: torch.randn(*s, dtype=torch.bfloat16) * 0.4)(S, Df) * 0.02),
            "w_i": _w((lambda *s: torch.randn(*s, dtype=torch.bfloat16) * 0.4)(S, Df) * 0.02),
        }
    _A_in = _A_setup()

    def _B_setup():
        B, S, D, F = 1, 2048, 512, 1792
        return {
            "x": _w((lambda *s: torch.randn(*s, dtype=torch.bfloat16) * 0.05)(B, S, D)),
            "w1": _w((lambda *s: torch.randn(*s, dtype=torch.bfloat16) * 0.4)(D, F) * (D ** -0.5)),
            "b1": _w((lambda *s: torch.randn(*s, dtype=torch.bfloat16) * 12.8)(F) * 0.01),
            "w2": _w((lambda *s: torch.randn(*s, dtype=torch.bfloat16) * 0.1)(F, D) * (F ** -0.5)),
            "b2": _w((lambda *s: torch.randn(*s, dtype=torch.bfloat16) * 0.8)(D) * 0.01),
        }
    _B_in = _B_setup()

    return {
        "A_x": _A_in["x"],
        "A_w_r": _A_in["w_r"],
        "A_w_i": _A_in["w_i"],
        "B_x": _B_in["x"],
        "B_w1": _B_in["w1"],
        "B_b1": _B_in["b1"],
        "B_w2": _B_in["w2"],
        "B_b2": _B_in["b2"],
    }

def reference(A_x, A_w_r, A_w_i, B_x, B_w1, B_b1, B_w2, B_b2):
    def _A_ref(x, w_r, w_i):
        B, S, D = x.shape
        X = (lambda x, axes=(-2,-1): torch.fft.rfft2(x.float(), dim=axes))(x, axes=(-2, -1))
        W = w_r + 1j * w_i
        X = X * W
        y = (lambda x, s, axes=(-2,-1): torch.fft.irfft2(x, s=s, dim=axes).to(torch.bfloat16))(X, (S, D), axes=(-2, -1))
        return y

    def _B_ref(x, w1, b1, w2, b2):
        h = torch.einsum("bsd,df->bsf", x, w1) + b1
        h = _gelu(h)
        return torch.einsum("bsf,fd->bsd", h, w2) + b2

    x = A_x
    x = x + _A_ref(_layernorm(x, axis=-1), A_w_r, A_w_i)
    x = x + _B_ref(_layernorm(x, axis=-1), B_w1, B_b1, B_w2, B_b2)
    x = x + _A_ref(_layernorm(x, axis=-1), A_w_r, A_w_i)
    x = x + _B_ref(_layernorm(x, axis=-1), B_w1, B_b1, B_w2, B_b2)
    return x
```

```cpp
#define OPTM 0
#define OPTW 192
#define WA0 0
#define WA1 8
#include <hip/hip_runtime.h>
#include <stdint.h>

typedef float f2 __attribute__((ext_vector_type(2)));
typedef float f4 __attribute__((ext_vector_type(4)));
typedef float f16v __attribute__((ext_vector_type(16)));
typedef unsigned u2 __attribute__((ext_vector_type(2)));
typedef unsigned u4 __attribute__((ext_vector_type(4)));
typedef short bf16x8 __attribute__((ext_vector_type(8)));
typedef __bf16 bfp2 __attribute__((ext_vector_type(2)));
typedef unsigned short u16;
typedef unsigned char u8;
struct __attribute__((aligned(8))) float2c { float x, y; };
#define GAS __attribute__((address_space(1)))

__device__ const float2c TWL[128] __attribute__((aligned(64))) = {
{1e+00f,-0e+00f},{1e+00f,-0e+00f},{1e+00f,-0e+00f},{0e+00f,0e+00f},{9.996988e-01f,-2.4541229e-02f},{9.8078525e-01f,-1.9509032e-01f},
{9.8078525e-01f,-1.9509032e-01f},{0e+00f,0e+00f},{9.9879545e-01f,-4.9067676e-02f},{9.238795e-01f,-3.8268343e-01f},{9.238795e-01f,-3.8268343e-01f},
{0e+00f,0e+00f},{9.9729043e-01f,-7.356457e-02f},{8.314696e-01f,-5.5557024e-01f},{8.314696e-01f,-5.5557024e-01f},{0e+00f,0e+00f},
{9.951847e-01f,-9.801714e-02f},{1e+00f,-0e+00f},{9.999247e-01f,-1.2271538e-02f},{0e+00f,0e+00f},{9.9247956e-01f,-1.2241068e-01f},
{9.8078525e-01f,-1.9509032e-01f},{9.783174e-01f,-2.0711137e-01f},{0e+00f,0e+00f},{9.891765e-01f,-1.4673047e-01f},{9.238795e-01f,-3.8268343e-01f},
{9.191139e-01f,-3.9399204e-01f},{0e+00f,0e+00f},{9.8527765e-01f,-1.7096189e-01f},{8.314696e-01f,-5.5557024e-01f},{8.245893e-01f,-5.657318e-01f},
{0e+00f,0e+00f},{9.8078525e-01f,-1.9509032e-01f},{1e+00f,-0e+00f},{9.996988e-01f,-2.4541229e-02f},{0e+00f,0e+00f},{9.757021e-01f,-2.1910124e-01f},
{9.8078525e-01f,-1.9509032e-01f},{9.757021e-01f,-2.1910124e-01f},{0e+00f,0e+00f},{9.7003126e-01f,-2.4298018e-01f},{9.238795e-01f,-3.8268343e-01f},
{9.142098e-01f,-4.052413e-01f},{0e+00f,0e+00f},{9.6377605e-01f,-2.6671275e-01f},{8.314696e-01f,-5.5557024e-01f},{8.175848e-01f,-5.7580817e-01f},
{0e+00f,0e+00f},{9.5694035e-01f,-2.9028466e-01f},{1e+00f,-0e+00f},{9.9932235e-01f,-3.6807224e-02f},{0e+00f,0e+00f},{9.4952816e-01f,-3.1368175e-01f},
{9.8078525e-01f,-1.9509032e-01f},{9.7293997e-01f,-2.310581e-01f},{0e+00f,0e+00f},{9.4154406e-01f,-3.3688986e-01f},{9.238795e-01f,-3.8268343e-01f},
{9.09168e-01f,-4.1642955e-01f},{0e+00f,0e+00f},{9.329928e-01f,-3.5989505e-01f},{8.314696e-01f,-5.5557024e-01f},{8.1045717e-01f,-5.8579785e-01f},
{0e+00f,0e+00f},{9.238795e-01f,-3.8268343e-01f},{1e+00f,-0e+00f},{9.9879545e-01f,-4.9067676e-02f},{0e+00f,0e+00f},{9.142098e-01f,-4.052413e-01f},
{9.8078525e-01f,-1.9509032e-01f},{9.7003126e-01f,-2.4298018e-01f},{0e+00f,0e+00f},{9.039893e-01f,-4.2755508e-01f},{9.238795e-01f,-3.8268343e-01f},
{9.039893e-01f,-4.2755508e-01f},{0e+00f,0e+00f},{8.932243e-01f,-4.4961134e-01f},{8.314696e-01f,-5.5557024e-01f},{8.032075e-01f,-5.956993e-01f},
{0e+00f,0e+00f},{8.819213e-01f,-4.7139674e-01f},{1e+00f,-0e+00f},{9.981181e-01f,-6.1320737e-02f},{0e+00f,0e+00f},{8.7008697e-01f,-4.928982e-01f},
{9.8078525e-01f,-1.9509032e-01f},{9.6697646e-01f,-2.5486565e-01f},{0e+00f,0e+00f},{8.577286e-01f,-5.1410276e-01f},{9.238795e-01f,-3.8268343e-01f},
{8.986745e-01f,-4.3861625e-01f},{0e+00f,0e+00f},{8.448536e-01f,-5.3499764e-01f},{8.314696e-01f,-5.5557024e-01f},{7.958369e-01f,-6.0551107e-01f},
{0e+00f,0e+00f},{8.314696e-01f,-5.5557024e-01f},{1e+00f,-0e+00f},{9.9729043e-01f,-7.356457e-02f},{0e+00f,0e+00f},{8.175848e-01f,-5.7580817e-01f},
{9.8078525e-01f,-1.9509032e-01f},{9.6377605e-01f,-2.6671275e-01f},{0e+00f,0e+00f},{8.032075e-01f,-5.956993e-01f},{9.238795e-01f,-3.8268343e-01f},
{8.932243e-01f,-4.4961134e-01f},{0e+00f,0e+00f},{7.883464e-01f,-6.152316e-01f},{8.314696e-01f,-5.5557024e-01f},{7.883464e-01f,-6.152316e-01f},
{0e+00f,0e+00f},{7.7301043e-01f,-6.343933e-01f},{1e+00f,-0e+00f},{9.963126e-01f,-8.579731e-02f},{0e+00f,0e+00f},{7.572088e-01f,-6.5317285e-01f},
{9.8078525e-01f,-1.9509032e-01f},{9.604305e-01f,-2.785197e-01f},{0e+00f,0e+00f},{7.409511e-01f,-6.71559e-01f},{9.238795e-01f,-3.8268343e-01f},
{8.8763964e-01f,-4.6053872e-01f},{0e+00f,0e+00f},{7.242471e-01f,-6.8954057e-01f},{8.314696e-01f,-5.5557024e-01f},{7.807372e-01f,-6.248595e-01f},
{0e+00f,0e+00f},
};
__device__ const float2c TW512[256] __attribute__((aligned(64))) = {
{1e+00f,-0e+00f},{9.999247e-01f,-1.2271538e-02f},{9.996988e-01f,-2.4541229e-02f},{9.9932235e-01f,-3.6807224e-02f},{9.9879545e-01f,-4.9067676e-02f},
{9.981181e-01f,-6.1320737e-02f},{9.9729043e-01f,-7.356457e-02f},{9.963126e-01f,-8.579731e-02f},{9.951847e-01f,-9.801714e-02f},
{9.93907e-01f,-1.10222206e-01f},{9.9247956e-01f,-1.2241068e-01f},{9.9090266e-01f,-1.345807e-01f},{9.891765e-01f,-1.4673047e-01f},
{9.873014e-01f,-1.5885815e-01f},{9.8527765e-01f,-1.7096189e-01f},{9.831055e-01f,-1.8303989e-01f},{9.8078525e-01f,-1.9509032e-01f},
{9.783174e-01f,-2.0711137e-01f},{9.757021e-01f,-2.1910124e-01f},{9.7293997e-01f,-2.310581e-01f},{9.7003126e-01f,-2.4298018e-01f},
{9.6697646e-01f,-2.5486565e-01f},{9.6377605e-01f,-2.6671275e-01f},{9.604305e-01f,-2.785197e-01f},{9.5694035e-01f,-2.9028466e-01f},
{9.53306e-01f,-3.0200595e-01f},{9.4952816e-01f,-3.1368175e-01f},{9.456073e-01f,-3.253103e-01f},{9.4154406e-01f,-3.3688986e-01f},
{9.37339e-01f,-3.4841868e-01f},{9.329928e-01f,-3.5989505e-01f},{9.285061e-01f,-3.713172e-01f},{9.238795e-01f,-3.8268343e-01f},
{9.191139e-01f,-3.9399204e-01f},{9.142098e-01f,-4.052413e-01f},{9.09168e-01f,-4.1642955e-01f},{9.039893e-01f,-4.2755508e-01f},
{8.986745e-01f,-4.3861625e-01f},{8.932243e-01f,-4.4961134e-01f},{8.8763964e-01f,-4.6053872e-01f},{8.819213e-01f,-4.7139674e-01f},
{8.760701e-01f,-4.8218378e-01f},{8.7008697e-01f,-4.928982e-01f},{8.6397284e-01f,-5.0353837e-01f},{8.577286e-01f,-5.1410276e-01f},
{8.513552e-01f,-5.2458966e-01f},{8.448536e-01f,-5.3499764e-01f},{8.382247e-01f,-5.45325e-01f},{8.314696e-01f,-5.5557024e-01f},
{8.245893e-01f,-5.657318e-01f},{8.175848e-01f,-5.7580817e-01f},{8.1045717e-01f,-5.8579785e-01f},{8.032075e-01f,-5.956993e-01f},
{7.958369e-01f,-6.0551107e-01f},{7.883464e-01f,-6.152316e-01f},{7.807372e-01f,-6.248595e-01f},{7.7301043e-01f,-6.343933e-01f},
{7.6516724e-01f,-6.4383155e-01f},{7.572088e-01f,-6.5317285e-01f},{7.491364e-01f,-6.624158e-01f},{7.409511e-01f,-6.71559e-01f},
{7.326543e-01f,-6.80601e-01f},{7.242471e-01f,-6.8954057e-01f},{7.1573085e-01f,-6.9837624e-01f},{7.0710677e-01f,-7.0710677e-01f},
{6.9837624e-01f,-7.1573085e-01f},{6.8954057e-01f,-7.242471e-01f},{6.80601e-01f,-7.326543e-01f},{6.71559e-01f,-7.409511e-01f},
{6.624158e-01f,-7.491364e-01f},{6.5317285e-01f,-7.572088e-01f},{6.4383155e-01f,-7.6516724e-01f},{6.343933e-01f,-7.7301043e-01f},
{6.248595e-01f,-7.807372e-01f},{6.152316e-01f,-7.883464e-01f},{6.0551107e-01f,-7.958369e-01f},{5.956993e-01f,-8.032075e-01f},
{5.8579785e-01f,-8.1045717e-01f},{5.7580817e-01f,-8.175848e-01f},{5.657318e-01f,-8.245893e-01f},{5.5557024e-01f,-8.314696e-01f},
{5.45325e-01f,-8.382247e-01f},{5.3499764e-01f,-8.448536e-01f},{5.2458966e-01f,-8.513552e-01f},{5.1410276e-01f,-8.577286e-01f},
{5.0353837e-01f,-8.6397284e-01f},{4.928982e-01f,-8.7008697e-01f},{4.8218378e-01f,-8.760701e-01f},{4.7139674e-01f,-8.819213e-01f},
{4.6053872e-01f,-8.8763964e-01f},{4.4961134e-01f,-8.932243e-01f},{4.3861625e-01f,-8.986745e-01f},{4.2755508e-01f,-9.039893e-01f},
{4.1642955e-01f,-9.09168e-01f},{4.052413e-01f,-9.142098e-01f},{3.9399204e-01f,-9.191139e-01f},{3.8268343e-01f,-9.238795e-01f},
{3.713172e-01f,-9.285061e-01f},{3.5989505e-01f,-9.329928e-01f},{3.4841868e-01f,-9.37339e-01f},{3.3688986e-01f,-9.4154406e-01f},
{3.253103e-01f,-9.456073e-01f},{3.1368175e-01f,-9.4952816e-01f},{3.0200595e-01f,-9.53306e-01f},{2.9028466e-01f,-9.5694035e-01f},
{2.785197e-01f,-9.604305e-01f},{2.6671275e-01f,-9.6377605e-01f},{2.5486565e-01f,-9.6697646e-01f},{2.4298018e-01f,-9.7003126e-01f},
{2.310581e-01f,-9.7293997e-01f},{2.1910124e-01f,-9.757021e-01f},{2.0711137e-01f,-9.783174e-01f},{1.9509032e-01f,-9.8078525e-01f},
{1.8303989e-01f,-9.831055e-01f},{1.7096189e-01f,-9.8527765e-01f},{1.5885815e-01f,-9.873014e-01f},{1.4673047e-01f,-9.891765e-01f},
{1.345807e-01f,-9.9090266e-01f},{1.2241068e-01f,-9.9247956e-01f},{1.10222206e-01f,-9.93907e-01f},{9.801714e-02f,-9.951847e-01f},
{8.579731e-02f,-9.963126e-01f},{7.356457e-02f,-9.9729043e-01f},{6.1320737e-02f,-9.981181e-01f},{4.9067676e-02f,-9.9879545e-01f},
{3.6807224e-02f,-9.9932235e-01f},{2.4541229e-02f,-9.996988e-01f},{1.2271538e-02f,-9.999247e-01f},{6.123234e-17f,-1e+00f},
{-1.2271538e-02f,-9.999247e-01f},{-2.4541229e-02f,-9.996988e-01f},{-3.6807224e-02f,-9.9932235e-01f},{-4.9067676e-02f,-9.9879545e-01f},
{-6.1320737e-02f,-9.981181e-01f},{-7.356457e-02f,-9.9729043e-01f},{-8.579731e-02f,-9.963126e-01f},{-9.801714e-02f,-9.951847e-01f},
{-1.10222206e-01f,-9.93907e-01f},{-1.2241068e-01f,-9.9247956e-01f},{-1.345807e-01f,-9.9090266e-01f},{-1.4673047e-01f,-9.891765e-01f},
{-1.5885815e-01f,-9.873014e-01f},{-1.7096189e-01f,-9.8527765e-01f},{-1.8303989e-01f,-9.831055e-01f},{-1.9509032e-01f,-9.8078525e-01f},
{-2.0711137e-01f,-9.783174e-01f},{-2.1910124e-01f,-9.757021e-01f},{-2.310581e-01f,-9.7293997e-01f},{-2.4298018e-01f,-9.7003126e-01f},
{-2.5486565e-01f,-9.6697646e-01f},{-2.6671275e-01f,-9.6377605e-01f},{-2.785197e-01f,-9.604305e-01f},{-2.9028466e-01f,-9.5694035e-01f},
{-3.0200595e-01f,-9.53306e-01f},{-3.1368175e-01f,-9.4952816e-01f},{-3.253103e-01f,-9.456073e-01f},{-3.3688986e-01f,-9.4154406e-01f},
{-3.4841868e-01f,-9.37339e-01f},{-3.5989505e-01f,-9.329928e-01f},{-3.713172e-01f,-9.285061e-01f},{-3.8268343e-01f,-9.238795e-01f},
{-3.9399204e-01f,-9.191139e-01f},{-4.052413e-01f,-9.142098e-01f},{-4.1642955e-01f,-9.09168e-01f},{-4.2755508e-01f,-9.039893e-01f},
{-4.3861625e-01f,-8.986745e-01f},{-4.4961134e-01f,-8.932243e-01f},{-4.6053872e-01f,-8.8763964e-01f},{-4.7139674e-01f,-8.819213e-01f},
{-4.8218378e-01f,-8.760701e-01f},{-4.928982e-01f,-8.7008697e-01f},{-5.0353837e-01f,-8.6397284e-01f},{-5.1410276e-01f,-8.577286e-01f},
{-5.2458966e-01f,-8.513552e-01f},{-5.3499764e-01f,-8.448536e-01f},{-5.45325e-01f,-8.382247e-01f},{-5.5557024e-01f,-8.314696e-01f},
{-5.657318e-01f,-8.245893e-01f},{-5.7580817e-01f,-8.175848e-01f},{-5.8579785e-01f,-8.1045717e-01f},{-5.956993e-01f,-8.032075e-01f},
{-6.0551107e-01f,-7.958369e-01f},{-6.152316e-01f,-7.883464e-01f},{-6.248595e-01f,-7.807372e-01f},{-6.343933e-01f,-7.7301043e-01f},
{-6.4383155e-01f,-7.6516724e-01f},{-6.5317285e-01f,-7.572088e-01f},{-6.624158e-01f,-7.491364e-01f},{-6.71559e-01f,-7.409511e-01f},
{-6.80601e-01f,-7.326543e-01f},{-6.8954057e-01f,-7.242471e-01f},{-6.9837624e-01f,-7.1573085e-01f},{-7.0710677e-01f,-7.0710677e-01f},
{-7.1573085e-01f,-6.9837624e-01f},{-7.242471e-01f,-6.8954057e-01f},{-7.326543e-01f,-6.80601e-01f},{-7.409511e-01f,-6.71559e-01f},
{-7.491364e-01f,-6.624158e-01f},{-7.572088e-01f,-6.5317285e-01f},{-7.6516724e-01f,-6.4383155e-01f},{-7.7301043e-01f,-6.343933e-01f},
{-7.807372e-01f,-6.248595e-01f},{-7.883464e-01f,-6.152316e-01f},{-7.958369e-01f,-6.0551107e-01f},{-8.032075e-01f,-5.956993e-01f},
{-8.1045717e-01f,-5.8579785e-01f},{-8.175848e-01f,-5.7580817e-01f},{-8.245893e-01f,-5.657318e-01f},{-8.314696e-01f,-5.5557024e-01f},
{-8.382247e-01f,-5.45325e-01f},{-8.448536e-01f,-5.3499764e-01f},{-8.513552e-01f,-5.2458966e-01f},{-8.577286e-01f,-5.1410276e-01f},
{-8.6397284e-01f,-5.0353837e-01f},{-8.7008697e-01f,-4.928982e-01f},{-8.760701e-01f,-4.8218378e-01f},{-8.819213e-01f,-4.7139674e-01f},
{-8.8763964e-01f,-4.6053872e-01f},{-8.932243e-01f,-4.4961134e-01f},{-8.986745e-01f,-4.3861625e-01f},{-9.039893e-01f,-4.2755508e-01f},
{-9.09168e-01f,-4.1642955e-01f},{-9.142098e-01f,-4.052413e-01f},{-9.191139e-01f,-3.9399204e-01f},{-9.238795e-01f,-3.8268343e-01f},
{-9.285061e-01f,-3.713172e-01f},{-9.329928e-01f,-3.5989505e-01f},{-9.37339e-01f,-3.4841868e-01f},{-9.4154406e-01f,-3.3688986e-01f},
{-9.456073e-01f,-3.253103e-01f},{-9.4952816e-01f,-3.1368175e-01f},{-9.53306e-01f,-3.0200595e-01f},{-9.5694035e-01f,-2.9028466e-01f},
{-9.604305e-01f,-2.785197e-01f},{-9.6377605e-01f,-2.6671275e-01f},{-9.6697646e-01f,-2.5486565e-01f},{-9.7003126e-01f,-2.4298018e-01f},
{-9.7293997e-01f,-2.310581e-01f},{-9.757021e-01f,-2.1910124e-01f},{-9.783174e-01f,-2.0711137e-01f},{-9.8078525e-01f,-1.9509032e-01f},
{-9.831055e-01f,-1.8303989e-01f},{-9.8527765e-01f,-1.7096189e-01f},{-9.873014e-01f,-1.5885815e-01f},{-9.891765e-01f,-1.4673047e-01f},
{-9.9090266e-01f,-1.345807e-01f},{-9.9247956e-01f,-1.2241068e-01f},{-9.93907e-01f,-1.10222206e-01f},{-9.951847e-01f,-9.801714e-02f},
{-9.963126e-01f,-8.579731e-02f},{-9.9729043e-01f,-7.356457e-02f},{-9.981181e-01f,-6.1320737e-02f},{-9.9879545e-01f,-4.9067676e-02f},
{-9.9932235e-01f,-3.6807224e-02f},{-9.996988e-01f,-2.4541229e-02f},{-9.999247e-01f,-1.2271538e-02f},
};
__device__ const float2c TWS[2048] __attribute__((aligned(64))) = {
{1e+00f,-0e+00f},{1e+00f,-0e+00f},{1e+00f,-0e+00f},{1e+00f,-0e+00f},{1e+00f,-0e+00f},{1e+00f,-0e+00f},{1e+00f,-0e+00f},{1e+00f,-0e+00f},
{1e+00f,-0e+00f},{9.999953e-01f,-3.0679568e-03f},{9.9998116e-01f,-6.1358847e-03f},{9.999576e-01f,-9.203754e-03f},{9.999247e-01f,-1.2271538e-02f},
{9.9988234e-01f,-1.5339206e-02f},{9.998306e-01f,-1.840673e-02f},{9.997694e-01f,-2.147408e-02f},{1e+00f,-0e+00f},{9.9998116e-01f,-6.1358847e-03f},
{9.999247e-01f,-1.2271538e-02f},{9.998306e-01f,-1.840673e-02f},{9.996988e-01f,-2.4541229e-02f},{9.995294e-01f,-3.0674804e-02f},
{9.9932235e-01f,-3.6807224e-02f},{9.9907774e-01f,-4.293826e-02f},{1e+00f,-0e+00f},{9.999576e-01f,-9.203754e-03f},{9.998306e-01f,-1.840673e-02f},
{9.996188e-01f,-2.7608145e-02f},{9.9932235e-01f,-3.6807224e-02f},{9.989413e-01f,-4.600318e-02f},{9.9847555e-01f,-5.5195246e-02f},
{9.979253e-01f,-6.438263e-02f},{1e+00f,-0e+00f},{9.999247e-01f,-1.2271538e-02f},{9.996988e-01f,-2.4541229e-02f},{9.9932235e-01f,-3.6807224e-02f},
{9.9879545e-01f,-4.9067676e-02f},{9.981181e-01f,-6.1320737e-02f},{9.9729043e-01f,-7.356457e-02f},{9.963126e-01f,-8.579731e-02f},{1e+00f,-0e+00f},
{9.9988234e-01f,-1.5339206e-02f},{9.995294e-01f,-3.0674804e-02f},{9.989413e-01f,-4.600318e-02f},{9.981181e-01f,-6.1320737e-02f},
{9.9706006e-01f,-7.6623864e-02f},{9.957674e-01f,-9.1908954e-02f},{9.9424046e-01f,-1.0717242e-01f},{1e+00f,-0e+00f},{9.998306e-01f,-1.840673e-02f},
{9.9932235e-01f,-3.6807224e-02f},{9.9847555e-01f,-5.5195246e-02f},{9.9729043e-01f,-7.356457e-02f},{9.957674e-01f,-9.1908954e-02f},
{9.93907e-01f,-1.10222206e-01f},{9.9170977e-01f,-1.284981e-01f},{1e+00f,-0e+00f},{9.997694e-01f,-2.147408e-02f},{9.9907774e-01f,-4.293826e-02f},
{9.979253e-01f,-6.438263e-02f},{9.963126e-01f,-8.579731e-02f},{9.9424046e-01f,-1.0717242e-01f},{9.9170977e-01f,-1.284981e-01f},
{9.8872167e-01f,-1.4976454e-01f},{1e+00f,-0e+00f},{9.996988e-01f,-2.4541229e-02f},{9.9879545e-01f,-4.9067676e-02f},{9.9729043e-01f,-7.356457e-02f},
{9.951847e-01f,-9.801714e-02f},{9.9247956e-01f,-1.2241068e-01f},{9.891765e-01f,-1.4673047e-01f},{9.8527765e-01f,-1.7096189e-01f},{1e+00f,-0e+00f},
{9.996188e-01f,-2.7608145e-02f},{9.9847555e-01f,-5.5195246e-02f},{9.965711e-01f,-8.274026e-02f},{9.93907e-01f,-1.10222206e-01f},
{9.904851e-01f,-1.3762012e-01f},{9.863081e-01f,-1.6491312e-01f},{9.813792e-01f,-1.920804e-01f},{1e+00f,-0e+00f},{9.995294e-01f,-3.0674804e-02f},
{9.981181e-01f,-6.1320737e-02f},{9.957674e-01f,-9.1908954e-02f},{9.9247956e-01f,-1.2241068e-01f},{9.882576e-01f,-1.5279719e-01f},
{9.831055e-01f,-1.8303989e-01f},{9.7702813e-01f,-2.1311031e-01f},{1e+00f,-0e+00f},{9.994306e-01f,-3.3741172e-02f},{9.9772304e-01f,-6.744392e-02f},
{9.948793e-01f,-1.0106986e-01f},{9.9090266e-01f,-1.345807e-01f},{9.857975e-01f,-1.6793829e-01f},{9.795698e-01f,-2.0110464e-01f},
{9.722265e-01f,-2.3404196e-01f},{1e+00f,-0e+00f},{9.9932235e-01f,-3.6807224e-02f},{9.9729043e-01f,-7.356457e-02f},{9.93907e-01f,-1.10222206e-01f},
{9.891765e-01f,-1.4673047e-01f},{9.831055e-01f,-1.8303989e-01f},{9.757021e-01f,-2.1910124e-01f},{9.6697646e-01f,-2.5486565e-01f},{1e+00f,-0e+00f},
{9.9920475e-01f,-3.9872926e-02f},{9.968203e-01f,-7.968244e-02f},{9.928504e-01f,-1.19365215e-01f},{9.873014e-01f,-1.5885815e-01f},
{9.801821e-01f,-1.980984e-01f},{9.715039e-01f,-2.370236e-01f},{9.6128047e-01f,-2.7557182e-01f},{1e+00f,-0e+00f},{9.9907774e-01f,-4.293826e-02f},
{9.963126e-01f,-8.579731e-02f},{9.9170977e-01f,-1.284981e-01f},{9.8527765e-01f,-1.7096189e-01f},{9.7702813e-01f,-2.1311031e-01f},
{9.6697646e-01f,-2.5486565e-01f},{9.551412e-01f,-2.961509e-01f},{1e+00f,-0e+00f},{9.989413e-01f,-4.600318e-02f},{9.957674e-01f,-9.1908954e-02f},
{9.904851e-01f,-1.3762012e-01f},{9.831055e-01f,-1.8303989e-01f},{9.7364426e-01f,-2.2807208e-01f},{9.621214e-01f,-2.7262136e-01f},
{9.485614e-01f,-3.1659338e-01f},{1e+00f,-0e+00f},{9.9879545e-01f,-4.9067676e-02f},{9.951847e-01f,-9.801714e-02f},{9.891765e-01f,-1.4673047e-01f},
{9.8078525e-01f,-1.9509032e-01f},{9.7003126e-01f,-2.4298018e-01f},{9.5694035e-01f,-2.9028466e-01f},{9.4154406e-01f,-3.3688986e-01f},{1e+00f,-0e+00f},
{9.9864024e-01f,-5.2131705e-02f},{9.945646e-01f,-1.0412163e-01f},{9.8778415e-01f,-1.558284e-01f},{9.783174e-01f,-2.0711137e-01f},
{9.6619e-01f,-2.578311e-01f},{9.51435e-01f,-3.0784965e-01f},{9.340925e-01f,-3.5703096e-01f},{1e+00f,-0e+00f},{9.9847555e-01f,-5.5195246e-02f},
{9.93907e-01f,-1.10222206e-01f},{9.863081e-01f,-1.6491312e-01f},{9.757021e-01f,-2.1910124e-01f},{9.621214e-01f,-2.7262136e-01f},
{9.456073e-01f,-3.253103e-01f},{9.262102e-01f,-3.7700742e-01f},{1e+00f,-0e+00f},{9.9830157e-01f,-5.8258265e-02f},{9.932119e-01f,-1.1631863e-01f},
{9.847485e-01f,-1.7398387e-01f},{9.7293997e-01f,-2.310581e-01f},{9.5782644e-01f,-2.8734747e-01f},{9.394592e-01f,-3.4266073e-01f},
{9.179008e-01f,-3.9681e-01f},{1e+00f,-0e+00f},{9.981181e-01f,-6.1320737e-02f},{9.9247956e-01f,-1.2241068e-01f},{9.831055e-01f,-1.8303989e-01f},
{9.7003126e-01f,-2.4298018e-01f},{9.53306e-01f,-3.0200595e-01f},{9.329928e-01f,-3.5989505e-01f},{9.09168e-01f,-4.1642955e-01f},{1e+00f,-0e+00f},
{9.979253e-01f,-6.438263e-02f},{9.9170977e-01f,-1.284981e-01f},{9.813792e-01f,-1.920804e-01f},{9.6697646e-01f,-2.5486565e-01f},
{9.485614e-01f,-3.1659338e-01f},{9.262102e-01f,-3.7700742e-01f},{9.000159e-01f,-4.358571e-01f},{1e+00f,-0e+00f},{9.9772304e-01f,-6.744392e-02f},
{9.9090266e-01f,-1.345807e-01f},{9.795698e-01f,-2.0110464e-01f},{9.6377605e-01f,-2.6671275e-01f},{9.4359344e-01f,-3.311063e-01f},
{9.191139e-01f,-3.9399204e-01f},{8.9044875e-01f,-4.5508358e-01f},{1e+00f,-0e+00f},{9.9751145e-01f,-7.0504576e-02f},{9.900582e-01f,-1.4065824e-01f},
{9.7767735e-01f,-2.1011184e-01f},{9.604305e-01f,-2.785197e-01f},{9.3840355e-01f,-3.4554133e-01f},{9.1170603e-01f,-4.1084316e-01f},
{8.804709e-01f,-4.741002e-01f},{1e+00f,-0e+00f},{9.9729043e-01f,-7.356457e-02f},{9.891765e-01f,-1.4673047e-01f},{9.757021e-01f,-2.1910124e-01f},
{9.5694035e-01f,-2.9028466e-01f},{9.329928e-01f,-3.5989505e-01f},{9.039893e-01f,-4.2755508e-01f},{8.7008697e-01f,-4.928982e-01f},{1e+00f,-0e+00f},
{9.9706006e-01f,-7.6623864e-02f},{9.882576e-01f,-1.5279719e-01f},{9.7364426e-01f,-2.2807208e-01f},{9.53306e-01f,-3.0200595e-01f},
{9.273625e-01f,-3.7416407e-01f},{8.9596623e-01f,-4.4412214e-01f},{8.593018e-01f,-5.114688e-01f},{1e+00f,-0e+00f},{9.968203e-01f,-7.968244e-02f},
{9.873014e-01f,-1.5885815e-01f},{9.715039e-01f,-2.370236e-01f},{9.4952816e-01f,-3.1368175e-01f},{9.2151403e-01f,-3.8834503e-01f},
{8.8763964e-01f,-4.6053872e-01f},{8.4812033e-01f,-5.2980363e-01f},{1e+00f,-0e+00f},{9.965711e-01f,-8.274026e-02f},{9.863081e-01f,-1.6491312e-01f},
{9.6928126e-01f,-2.4595505e-01f},{9.456073e-01f,-3.253103e-01f},{9.154487e-01f,-4.0243465e-01f},{8.790122e-01f,-4.7679922e-01f},
{8.3654773e-01f,-5.4789406e-01f},{1e+00f,-0e+00f},{9.963126e-01f,-8.579731e-02f},{9.8527765e-01f,-1.7096189e-01f},{9.6697646e-01f,-2.5486565e-01f},
{9.4154406e-01f,-3.3688986e-01f},{9.09168e-01f,-4.1642955e-01f},{8.7008697e-01f,-4.928982e-01f},{8.245893e-01f,-5.657318e-01f},{1e+00f,-0e+00f},
{9.960447e-01f,-8.885355e-02f},{9.842101e-01f,-1.7700422e-01f},{9.645898e-01f,-2.6375467e-01f},{9.37339e-01f,-3.4841868e-01f},
{9.026733e-01f,-4.303265e-01f},{8.6086696e-01f,-5.0883013e-01f},{8.122506e-01f,-5.8330864e-01f},{1e+00f,-0e+00f},{9.957674e-01f,-9.1908954e-02f},
{9.831055e-01f,-1.8303989e-01f},{9.621214e-01f,-2.7262136e-01f},{9.329928e-01f,-3.5989505e-01f},{8.9596623e-01f,-4.4412214e-01f},
{8.513552e-01f,-5.2458966e-01f},{7.9953724e-01f,-6.0061646e-01f},{1e+00f,-0e+00f},{9.954808e-01f,-9.49635e-02f},{9.819639e-01f,-1.8906866e-01f},
{9.5957154e-01f,-2.8146493e-01f},{9.285061e-01f,-3.713172e-01f},{8.8904834e-01f,-4.578133e-01f},{8.41555e-01f,-5.4017144e-01f},
{7.864552e-01f,-6.176473e-01f},{1e+00f,-0e+00f},{9.951847e-01f,-9.801714e-02f},{9.8078525e-01f,-1.9509032e-01f},{9.5694035e-01f,-2.9028466e-01f},
{9.238795e-01f,-3.8268343e-01f},{8.819213e-01f,-4.7139674e-01f},{8.314696e-01f,-5.5557024e-01f},{7.7301043e-01f,-6.343933e-01f},{1e+00f,-0e+00f},
{9.948793e-01f,-1.0106986e-01f},{9.795698e-01f,-2.0110464e-01f},{9.542281e-01f,-2.9907984e-01f},{9.191139e-01f,-3.9399204e-01f},
{8.7458664e-01f,-4.8486924e-01f},{8.211025e-01f,-5.7078075e-01f},{7.592092e-01f,-6.5084666e-01f},{1e+00f,-0e+00f},{9.945646e-01f,-1.0412163e-01f},
{9.783174e-01f,-2.0711137e-01f},{9.51435e-01f,-3.0784965e-01f},{9.142098e-01f,-4.052413e-01f},{8.6704624e-01f,-4.9822766e-01f},
{8.1045717e-01f,-5.8579785e-01f},{7.4505776e-01f,-6.6699994e-01f},{1e+00f,-0e+00f},{9.9424046e-01f,-1.0717242e-01f},{9.7702813e-01f,-2.1311031e-01f},
{9.485614e-01f,-3.1659338e-01f},{9.09168e-01f,-4.1642955e-01f},{8.593018e-01f,-5.114688e-01f},{7.9953724e-01f,-6.0061646e-01f},
{7.3056275e-01f,-6.8284553e-01f},{1e+00f,-0e+00f},{9.93907e-01f,-1.10222206e-01f},{9.757021e-01f,-2.1910124e-01f},{9.456073e-01f,-3.253103e-01f},
{9.039893e-01f,-4.2755508e-01f},{8.513552e-01f,-5.2458966e-01f},{7.883464e-01f,-6.152316e-01f},{7.1573085e-01f,-6.9837624e-01f},{1e+00f,-0e+00f},
{9.935641e-01f,-1.1327095e-01f},{9.7433937e-01f,-2.2508392e-01f},{9.425732e-01f,-3.3399966e-01f},{8.986745e-01f,-4.3861625e-01f},
{8.4320825e-01f,-5.3758705e-01f},{7.768885e-01f,-6.2963825e-01f},{7.005688e-01f,-7.1358484e-01f},{1e+00f,-0e+00f},{9.932119e-01f,-1.1631863e-01f},
{9.7293997e-01f,-2.310581e-01f},{9.394592e-01f,-3.4266073e-01f},{8.932243e-01f,-4.4961134e-01f},{8.348629e-01f,-5.5045795e-01f},
{7.6516724e-01f,-6.4383155e-01f},{6.850837e-01f,-7.2846437e-01f},{1e+00f,-0e+00f},{9.928504e-01f,-1.19365215e-01f},{9.715039e-01f,-2.370236e-01f},
{9.3626565e-01f,-3.5129276e-01f},{8.8763964e-01f,-4.6053872e-01f},{8.2632107e-01f,-5.6319934e-01f},{7.531868e-01f,-6.578067e-01f},
{6.692826e-01f,-7.4300796e-01f},{1e+00f,-0e+00f},{9.9247956e-01f,-1.2241068e-01f},{9.7003126e-01f,-2.4298018e-01f},{9.329928e-01f,-3.5989505e-01f},
{8.819213e-01f,-4.7139674e-01f},{8.175848e-01f,-5.7580817e-01f},{7.409511e-01f,-6.71559e-01f},{6.5317285e-01f,-7.572088e-01f},{1e+00f,-0e+00f},
{9.920993e-01f,-1.2545498e-01f},{9.685221e-01f,-2.4892761e-01f},{9.296409e-01f,-3.6846682e-01f},{8.760701e-01f,-4.8218378e-01f},
{8.0865616e-01f,-5.882816e-01f},{7.2846437e-01f,-6.850837e-01f},{6.3676184e-01f,-7.710605e-01f},{1e+00f,-0e+00f},{9.9170977e-01f,-1.284981e-01f},
{9.6697646e-01f,-2.5486565e-01f},{9.262102e-01f,-3.7700742e-01f},{8.7008697e-01f,-4.928982e-01f},{7.9953724e-01f,-6.0061646e-01f},
{7.1573085e-01f,-6.9837624e-01f},{6.200572e-01f,-7.8455657e-01f},{1e+00f,-0e+00f},{9.9131083e-01f,-1.3154003e-01f},{9.6539444e-01f,-2.607941e-01f},
{9.227011e-01f,-3.8551605e-01f},{8.6397284e-01f,-5.0353837e-01f},{7.902302e-01f,-6.128101e-01f},{7.0275474e-01f,-7.114322e-01f},
{6.030666e-01f,-7.9769087e-01f},{1e+00f,-0e+00f},{9.9090266e-01f,-1.345807e-01f},{9.6377605e-01f,-2.6671275e-01f},{9.191139e-01f,-3.9399204e-01f},
{8.577286e-01f,-5.1410276e-01f},{7.807372e-01f,-6.248595e-01f},{6.8954057e-01f,-7.242471e-01f},{5.8579785e-01f,-8.1045717e-01f},{1e+00f,-0e+00f},
{9.904851e-01f,-1.3762012e-01f},{9.621214e-01f,-2.7262136e-01f},{9.154487e-01f,-4.0243465e-01f},{8.513552e-01f,-5.2458966e-01f},
{7.710605e-01f,-6.3676184e-01f},{6.760927e-01f,-7.368166e-01f},{5.6825894e-01f,-8.228498e-01f},{1e+00f,-0e+00f},{9.900582e-01f,-1.4065824e-01f},
{9.604305e-01f,-2.785197e-01f},{9.1170603e-01f,-4.1084316e-01f},{8.448536e-01f,-5.3499764e-01f},{7.612024e-01f,-6.485144e-01f},
{6.624158e-01f,-7.491364e-01f},{5.5045795e-01f,-8.348629e-01f},{1e+00f,-0e+00f},{9.89622e-01f,-1.4369503e-01f},{9.5870346e-01f,-2.8440753e-01f},
{9.078861e-01f,-4.192169e-01f},{8.382247e-01f,-5.45325e-01f},{7.5116515e-01f,-6.6011435e-01f},{6.485144e-01f,-7.612024e-01f},
{5.324031e-01f,-8.464909e-01f},{1e+00f,-0e+00f},{9.891765e-01f,-1.4673047e-01f},{9.5694035e-01f,-2.9028466e-01f},{9.039893e-01f,-4.2755508e-01f},
{8.314696e-01f,-5.5557024e-01f},{7.409511e-01f,-6.71559e-01f},{6.343933e-01f,-7.7301043e-01f},{5.1410276e-01f,-8.577286e-01f},{1e+00f,-0e+00f},
{9.8872167e-01f,-1.4976454e-01f},{9.551412e-01f,-2.961509e-01f},{9.000159e-01f,-4.358571e-01f},{8.245893e-01f,-5.657318e-01f},
{7.3056275e-01f,-6.8284553e-01f},{6.200572e-01f,-7.8455657e-01f},{4.9556527e-01f,-8.685707e-01f},{1e+00f,-0e+00f},{9.882576e-01f,-1.5279719e-01f},
{9.53306e-01f,-3.0200595e-01f},{8.9596623e-01f,-4.4412214e-01f},{8.175848e-01f,-5.7580817e-01f},{7.2000253e-01f,-6.9397146e-01f},
{6.0551107e-01f,-7.958369e-01f},{4.7679922e-01f,-8.790122e-01f},{1e+00f,-0e+00f},{9.8778415e-01f,-1.558284e-01f},{9.51435e-01f,-3.0784965e-01f},
{8.918407e-01f,-4.5234957e-01f},{8.1045717e-01f,-5.8579785e-01f},{7.092728e-01f,-7.0493406e-01f},{5.907597e-01f,-8.068476e-01f},
{4.578133e-01f,-8.8904834e-01f},{1e+00f,-0e+00f},{9.873014e-01f,-1.5885815e-01f},{9.4952816e-01f,-3.1368175e-01f},{8.8763964e-01f,-4.6053872e-01f},
{8.032075e-01f,-5.956993e-01f},{6.9837624e-01f,-7.1573085e-01f},{5.7580817e-01f,-8.175848e-01f},{4.3861625e-01f,-8.986745e-01f},{1e+00f,-0e+00f},
{9.868094e-01f,-1.618864e-01f},{9.475856e-01f,-3.1950203e-01f},{8.8336337e-01f,-4.6868882e-01f},{7.958369e-01f,-6.0551107e-01f},
{6.8731534e-01f,-7.263591e-01f},{5.6066155e-01f,-8.2804507e-01f},{4.192169e-01f,-9.078861e-01f},{1e+00f,-0e+00f},{9.863081e-01f,-1.6491312e-01f},
{9.456073e-01f,-3.253103e-01f},{8.790122e-01f,-4.7679922e-01f},{7.883464e-01f,-6.152316e-01f},{6.760927e-01f,-7.368166e-01f},
{5.45325e-01f,-8.382247e-01f},{3.996242e-01f,-9.166791e-01f},{1e+00f,-0e+00f},{9.857975e-01f,-1.6793829e-01f},{9.4359344e-01f,-3.311063e-01f},
{8.7458664e-01f,-4.8486924e-01f},{7.807372e-01f,-6.248595e-01f},{6.64711e-01f,-7.471006e-01f},{5.2980363e-01f,-8.4812033e-01f},
{3.798472e-01f,-9.2504925e-01f},{1e+00f,-0e+00f},{9.8527765e-01f,-1.7096189e-01f},{9.4154406e-01f,-3.3688986e-01f},{8.7008697e-01f,-4.928982e-01f},
{7.7301043e-01f,-6.343933e-01f},{6.5317285e-01f,-7.572088e-01f},{5.1410276e-01f,-8.577286e-01f},{3.5989505e-01f,-9.329928e-01f},{1e+00f,-0e+00f},
{9.847485e-01f,-1.7398387e-01f},{9.394592e-01f,-3.4266073e-01f},{8.655136e-01f,-5.0088537e-01f},{7.6516724e-01f,-6.4383155e-01f},
{6.4148104e-01f,-7.671389e-01f},{4.9822766e-01f,-8.6704624e-01f},{3.3977687e-01f,-9.4050604e-01f},{1e+00f,-0e+00f},{9.842101e-01f,-1.7700422e-01f},
{9.37339e-01f,-3.4841868e-01f},{8.6086696e-01f,-5.0883013e-01f},{7.572088e-01f,-6.5317285e-01f},{6.2963825e-01f,-7.768885e-01f},
{4.8218378e-01f,-8.760701e-01f},{3.1950203e-01f,-9.475856e-01f},{1e+00f,-0e+00f},{9.836624e-01f,-1.800229e-01f},{9.351835e-01f,-3.5416353e-01f},
{8.5614735e-01f,-5.167318e-01f},{7.491364e-01f,-6.624158e-01f},{6.176473e-01f,-7.864552e-01f},{4.659765e-01f,-8.847971e-01f},
{2.9907984e-01f,-9.542281e-01f},{1e+00f,-0e+00f},{9.831055e-01f,-1.8303989e-01f},{9.329928e-01f,-3.5989505e-01f},{8.513552e-01f,-5.2458966e-01f},
{7.409511e-01f,-6.71559e-01f},{6.0551107e-01f,-7.958369e-01f},{4.4961134e-01f,-8.932243e-01f},{2.785197e-01f,-9.604305e-01f},{1e+00f,-0e+00f},
{9.825393e-01f,-1.8605515e-01f},{9.3076694e-01f,-3.6561298e-01f},{8.464909e-01f,-5.324031e-01f},{7.326543e-01f,-6.80601e-01f},
{5.932323e-01f,-8.0503136e-01f},{4.3309382e-01f,-9.013488e-01f},{2.578311e-01f,-9.6619e-01f},{1e+00f,-0e+00f},{9.819639e-01f,-1.8906866e-01f},
{9.285061e-01f,-3.713172e-01f},{8.41555e-01f,-5.4017144e-01f},{7.242471e-01f,-6.8954057e-01f},{5.8081394e-01f,-8.140363e-01f},
{4.1642955e-01f,-9.09168e-01f},{2.370236e-01f,-9.715039e-01f},{1e+00f,-0e+00f},{9.813792e-01f,-1.920804e-01f},{9.262102e-01f,-3.7700742e-01f},
{8.3654773e-01f,-5.4789406e-01f},{7.1573085e-01f,-6.9837624e-01f},{5.6825894e-01f,-8.228498e-01f},{3.996242e-01f,-9.166791e-01f},
{2.161068e-01f,-9.7636974e-01f},{1e+00f,-0e+00f},{9.8078525e-01f,-1.9509032e-01f},{9.238795e-01f,-3.8268343e-01f},{8.314696e-01f,-5.5557024e-01f},
{7.0710677e-01f,-7.0710677e-01f},{5.5557024e-01f,-8.314696e-01f},{3.8268343e-01f,-9.238795e-01f},{1.9509032e-01f,-9.8078525e-01f},{1e+00f,-0e+00f},
{9.801821e-01f,-1.980984e-01f},{9.2151403e-01f,-3.8834503e-01f},{8.2632107e-01f,-5.6319934e-01f},{6.9837624e-01f,-7.1573085e-01f},
{5.427508e-01f,-8.398938e-01f},{3.6561298e-01f,-9.3076694e-01f},{1.7398387e-01f,-9.847485e-01f},{1e+00f,-0e+00f},{9.795698e-01f,-2.0110464e-01f},
{9.191139e-01f,-3.9399204e-01f},{8.211025e-01f,-5.7078075e-01f},{6.8954057e-01f,-7.242471e-01f},{5.2980363e-01f,-8.4812033e-01f},
{3.4841868e-01f,-9.37339e-01f},{1.5279719e-01f,-9.882576e-01f},{1e+00f,-0e+00f},{9.789482e-01f,-2.0410897e-01f},{9.166791e-01f,-3.996242e-01f},
{8.1581444e-01f,-5.7831377e-01f},{6.80601e-01f,-7.326543e-01f},{5.167318e-01f,-8.5614735e-01f},{3.311063e-01f,-9.4359344e-01f},
{1.3154003e-01f,-9.9131083e-01f},{1e+00f,-0e+00f},{9.783174e-01f,-2.0711137e-01f},{9.142098e-01f,-4.052413e-01f},{8.1045717e-01f,-5.8579785e-01f},
{6.71559e-01f,-7.409511e-01f},{5.0353837e-01f,-8.6397284e-01f},{3.1368175e-01f,-9.4952816e-01f},{1.10222206e-01f,-9.93907e-01f},{1e+00f,-0e+00f},
{9.7767735e-01f,-2.1011184e-01f},{9.1170603e-01f,-4.1084316e-01f},{8.0503136e-01f,-5.932323e-01f},{6.624158e-01f,-7.491364e-01f},
{4.9022648e-01f,-8.715951e-01f},{2.961509e-01f,-9.551412e-01f},{8.885355e-02f,-9.960447e-01f},{1e+00f,-0e+00f},{9.7702813e-01f,-2.1311031e-01f},
{9.09168e-01f,-4.1642955e-01f},{7.9953724e-01f,-6.0061646e-01f},{6.5317285e-01f,-7.572088e-01f},{4.7679922e-01f,-8.790122e-01f},
{2.785197e-01f,-9.604305e-01f},{6.744392e-02f,-9.9772304e-01f},{1e+00f,-0e+00f},{9.7636974e-01f,-2.161068e-01f},{9.065957e-01f,-4.2200026e-01f},
{7.939755e-01f,-6.079498e-01f},{6.4383155e-01f,-7.6516724e-01f},{4.632598e-01f,-8.8622254e-01f},{2.607941e-01f,-9.6539444e-01f},
{4.600318e-02f,-9.989413e-01f},{1e+00f,-0e+00f},{9.757021e-01f,-2.1910124e-01f},{9.039893e-01f,-4.2755508e-01f},{7.883464e-01f,-6.152316e-01f},
{6.343933e-01f,-7.7301043e-01f},{4.4961134e-01f,-8.932243e-01f},{2.4298018e-01f,-9.7003126e-01f},{2.4541229e-02f,-9.996988e-01f},{1e+00f,-0e+00f},
{9.7502536e-01f,-2.2209363e-01f},{9.013488e-01f,-4.3309382e-01f},{7.826506e-01f,-6.2246126e-01f},{6.248595e-01f,-7.807372e-01f},
{4.358571e-01f,-9.000159e-01f},{2.2508392e-01f,-9.7433937e-01f},{3.0679568e-03f,-9.999953e-01f},{1e+00f,-0e+00f},{9.7433937e-01f,-2.2508392e-01f},
{8.986745e-01f,-4.3861625e-01f},{7.768885e-01f,-6.2963825e-01f},{6.152316e-01f,-7.883464e-01f},{4.2200026e-01f,-9.065957e-01f},
{2.0711137e-01f,-9.783174e-01f},{-1.840673e-02f,-9.998306e-01f},{1e+00f,-0e+00f},{9.7364426e-01f,-2.2807208e-01f},{8.9596623e-01f,-4.4412214e-01f},
{7.710605e-01f,-6.3676184e-01f},{6.0551107e-01f,-7.958369e-01f},{4.0804416e-01f,-9.129622e-01f},{1.8906866e-01f,-9.819639e-01f},
{-3.9872926e-02f,-9.9920475e-01f},{1e+00f,-0e+00f},{9.7293997e-01f,-2.310581e-01f},{8.932243e-01f,-4.4961134e-01f},{7.6516724e-01f,-6.4383155e-01f},
{5.956993e-01f,-8.032075e-01f},{3.9399204e-01f,-9.191139e-01f},{1.7096189e-01f,-9.8527765e-01f},{-6.1320737e-02f,-9.981181e-01f},{1e+00f,-0e+00f},
{9.722265e-01f,-2.3404196e-01f},{8.9044875e-01f,-4.5508358e-01f},{7.592092e-01f,-6.5084666e-01f},{5.8579785e-01f,-8.1045717e-01f},
{3.798472e-01f,-9.2504925e-01f},{1.5279719e-01f,-9.882576e-01f},{-8.274026e-02f,-9.965711e-01f},{1e+00f,-0e+00f},{9.715039e-01f,-2.370236e-01f},
{8.8763964e-01f,-4.6053872e-01f},{7.531868e-01f,-6.578067e-01f},{5.7580817e-01f,-8.175848e-01f},{3.6561298e-01f,-9.3076694e-01f},
{1.345807e-01f,-9.9090266e-01f},{-1.0412163e-01f,-9.945646e-01f},{1e+00f,-0e+00f},{9.7077215e-01f,-2.4000302e-01f},{8.847971e-01f,-4.659765e-01f},
{7.471006e-01f,-6.64711e-01f},{5.657318e-01f,-8.245893e-01f},{3.5129276e-01f,-9.3626565e-01f},{1.1631863e-01f,-9.932119e-01f},
{-1.2545498e-01f,-9.920993e-01f},{1e+00f,-0e+00f},{9.7003126e-01f,-2.4298018e-01f},{8.819213e-01f,-4.7139674e-01f},{7.409511e-01f,-6.71559e-01f},
{5.5557024e-01f,-8.314696e-01f},{3.3688986e-01f,-9.4154406e-01f},{9.801714e-02f,-9.951847e-01f},{-1.4673047e-01f,-9.891765e-01f},{1e+00f,-0e+00f},
{9.6928126e-01f,-2.4595505e-01f},{8.790122e-01f,-4.7679922e-01f},{7.347389e-01f,-6.7835003e-01f},{5.45325e-01f,-8.382247e-01f},
{3.224077e-01f,-9.466009e-01f},{7.968244e-02f,-9.968203e-01f},{-1.6793829e-01f,-9.857975e-01f},{1e+00f,-0e+00f},{9.685221e-01f,-2.4892761e-01f},
{8.760701e-01f,-4.8218378e-01f},{7.2846437e-01f,-6.850837e-01f},{5.3499764e-01f,-8.448536e-01f},{3.0784965e-01f,-9.51435e-01f},
{6.1320737e-02f,-9.981181e-01f},{-1.8906866e-01f,-9.819639e-01f},{1e+00f,-0e+00f},{9.677538e-01f,-2.518978e-01f},{8.73095e-01f,-4.8755017e-01f},
{7.221282e-01f,-6.917592e-01f},{5.2458966e-01f,-8.513552e-01f},{2.9321915e-01f,-9.5604527e-01f},{4.293826e-02f,-9.9907774e-01f},
{-2.1011184e-01f,-9.7767735e-01f},{1e+00f,-0e+00f},{9.6697646e-01f,-2.5486565e-01f},{8.7008697e-01f,-4.928982e-01f},{7.1573085e-01f,-6.9837624e-01f},
{5.1410276e-01f,-8.577286e-01f},{2.785197e-01f,-9.604305e-01f},{2.4541229e-02f,-9.996988e-01f},{-2.310581e-01f,-9.7293997e-01f},{1e+00f,-0e+00f},
{9.6619e-01f,-2.578311e-01f},{8.6704624e-01f,-4.9822766e-01f},{7.092728e-01f,-7.0493406e-01f},{5.0353837e-01f,-8.6397284e-01f},
{2.6375467e-01f,-9.645898e-01f},{6.1358847e-03f,-9.9998116e-01f},{-2.518978e-01f,-9.677538e-01f},{1e+00f,-0e+00f},{9.6539444e-01f,-2.607941e-01f},
{8.6397284e-01f,-5.0353837e-01f},{7.0275474e-01f,-7.114322e-01f},{4.928982e-01f,-8.7008697e-01f},{2.4892761e-01f,-9.685221e-01f},
{-1.2271538e-02f,-9.999247e-01f},{-2.7262136e-01f,-9.621214e-01f},{1e+00f,-0e+00f},{9.645898e-01f,-2.6375467e-01f},{8.6086696e-01f,-5.0883013e-01f},
{6.961771e-01f,-7.1787006e-01f},{4.8218378e-01f,-8.760701e-01f},{2.3404196e-01f,-9.722265e-01f},{-3.0674804e-02f,-9.995294e-01f},
{-2.9321915e-01f,-9.5604527e-01f},{1e+00f,-0e+00f},{9.6377605e-01f,-2.6671275e-01f},{8.577286e-01f,-5.1410276e-01f},{6.8954057e-01f,-7.242471e-01f},
{4.7139674e-01f,-8.819213e-01f},{2.1910124e-01f,-9.757021e-01f},{-4.9067676e-02f,-9.9879545e-01f},{-3.1368175e-01f,-9.4952816e-01f},{1e+00f,-0e+00f},
{9.6295327e-01f,-2.696683e-01f},{8.54558e-01f,-5.19356e-01f},{6.8284553e-01f,-7.3056275e-01f},{4.6053872e-01f,-8.8763964e-01f},
{2.0410897e-01f,-9.789482e-01f},{-6.744392e-02f,-9.9772304e-01f},{-3.3399966e-01f,-9.425732e-01f},{1e+00f,-0e+00f},{9.621214e-01f,-2.7262136e-01f},
{8.513552e-01f,-5.2458966e-01f},{6.760927e-01f,-7.368166e-01f},{4.4961134e-01f,-8.932243e-01f},{1.8906866e-01f,-9.819639e-01f},
{-8.579731e-02f,-9.963126e-01f},{-3.5416353e-01f,-9.351835e-01f},{1e+00f,-0e+00f},{9.6128047e-01f,-2.7557182e-01f},{8.4812033e-01f,-5.2980363e-01f},
{6.692826e-01f,-7.4300796e-01f},{4.3861625e-01f,-8.986745e-01f},{1.7398387e-01f,-9.847485e-01f},{-1.0412163e-01f,-9.945646e-01f},
{-3.7416407e-01f,-9.273625e-01f},{1e+00f,-0e+00f},{9.604305e-01f,-2.785197e-01f},{8.448536e-01f,-5.3499764e-01f},{6.624158e-01f,-7.491364e-01f},
{4.2755508e-01f,-9.039893e-01f},{1.5885815e-01f,-9.873014e-01f},{-1.2241068e-01f,-9.9247956e-01f},{-3.9399204e-01f,-9.191139e-01f},{1e+00f,-0e+00f},
{9.5957154e-01f,-2.8146493e-01f},{8.41555e-01f,-5.4017144e-01f},{6.5549284e-01f,-7.552014e-01f},{4.1642955e-01f,-9.09168e-01f},
{1.4369503e-01f,-9.89622e-01f},{-1.4065824e-01f,-9.900582e-01f},{-4.1363832e-01f,-9.104413e-01f},{1e+00f,-0e+00f},{9.5870346e-01f,-2.8440753e-01f},
{8.382247e-01f,-5.45325e-01f},{6.485144e-01f,-7.612024e-01f},{4.052413e-01f,-9.142098e-01f},{1.284981e-01f,-9.9170977e-01f},
{-1.5885815e-01f,-9.873014e-01f},{-4.3309382e-01f,-9.013488e-01f},{1e+00f,-0e+00f},{9.5782644e-01f,-2.8734747e-01f},{8.348629e-01f,-5.5045795e-01f},
{6.4148104e-01f,-7.671389e-01f},{3.9399204e-01f,-9.191139e-01f},{1.1327095e-01f,-9.935641e-01f},{-1.7700422e-01f,-9.842101e-01f},
{-4.5234957e-01f,-8.918407e-01f},{1e+00f,-0e+00f},{9.5694035e-01f,-2.9028466e-01f},{8.314696e-01f,-5.5557024e-01f},{6.343933e-01f,-7.7301043e-01f},
{3.8268343e-01f,-9.238795e-01f},{9.801714e-02f,-9.951847e-01f},{-1.9509032e-01f,-9.8078525e-01f},{-4.7139674e-01f,-8.819213e-01f},{1e+00f,-0e+00f},
{9.5604527e-01f,-2.9321915e-01f},{8.2804507e-01f,-5.6066155e-01f},{6.272518e-01f,-7.788165e-01f},{3.713172e-01f,-9.285061e-01f},
{8.274026e-02f,-9.965711e-01f},{-2.1311031e-01f,-9.7702813e-01f},{-4.9022648e-01f,-8.715951e-01f},{1e+00f,-0e+00f},{9.551412e-01f,-2.961509e-01f},
{8.245893e-01f,-5.657318e-01f},{6.200572e-01f,-7.8455657e-01f},{3.5989505e-01f,-9.329928e-01f},{6.744392e-02f,-9.9772304e-01f},
{-2.310581e-01f,-9.7293997e-01f},{-5.0883013e-01f,-8.6086696e-01f},{1e+00f,-0e+00f},{9.542281e-01f,-2.9907984e-01f},{8.211025e-01f,-5.7078075e-01f},
{6.128101e-01f,-7.902302e-01f},{3.4841868e-01f,-9.37339e-01f},{5.2131705e-02f,-9.9864024e-01f},{-2.4892761e-01f,-9.685221e-01f},
{-5.2719915e-01f,-8.4974176e-01f},{1e+00f,-0e+00f},{9.53306e-01f,-3.0200595e-01f},{8.175848e-01f,-5.7580817e-01f},{6.0551107e-01f,-7.958369e-01f},
{3.3688986e-01f,-9.4154406e-01f},{3.6807224e-02f,-9.9932235e-01f},{-2.6671275e-01f,-9.6377605e-01f},{-5.45325e-01f,-8.382247e-01f},{1e+00f,-0e+00f},
{9.52375e-01f,-3.0492923e-01f},{8.140363e-01f,-5.8081394e-01f},{5.981607e-01f,-8.0137616e-01f},{3.253103e-01f,-9.456073e-01f},
{2.147408e-02f,-9.997694e-01f},{-2.8440753e-01f,-9.5870346e-01f},{-5.6319934e-01f,-8.2632107e-01f},{1e+00f,-0e+00f},{9.51435e-01f,-3.0784965e-01f},
{8.1045717e-01f,-5.8579785e-01f},{5.907597e-01f,-8.068476e-01f},{3.1368175e-01f,-9.4952816e-01f},{6.1358847e-03f,-9.9998116e-01f},
{-3.0200595e-01f,-9.53306e-01f},{-5.8081394e-01f,-8.140363e-01f},{1e+00f,-0e+00f},{9.5048606e-01f,-3.1076714e-01f},{8.068476e-01f,-5.907597e-01f},
{5.8330864e-01f,-8.122506e-01f},{3.0200595e-01f,-9.53306e-01f},{-9.203754e-03f,-9.999576e-01f},{-3.1950203e-01f,-9.475856e-01f},
{-5.981607e-01f,-8.0137616e-01f},{1e+00f,-0e+00f},{9.4952816e-01f,-3.1368175e-01f},{8.032075e-01f,-5.956993e-01f},{5.7580817e-01f,-8.175848e-01f},
{2.9028466e-01f,-9.5694035e-01f},{-2.4541229e-02f,-9.996988e-01f},{-3.3688986e-01f,-9.4154406e-01f},{-6.152316e-01f,-7.883464e-01f},{1e+00f,-0e+00f},
{9.485614e-01f,-3.1659338e-01f},{7.9953724e-01f,-6.0061646e-01f},{5.6825894e-01f,-8.228498e-01f},{2.785197e-01f,-9.604305e-01f},
{-3.9872926e-02f,-9.9920475e-01f},{-3.5416353e-01f,-9.351835e-01f},{-6.3201874e-01f,-7.749531e-01f},{1e+00f,-0e+00f},{9.475856e-01f,-3.1950203e-01f},
{7.958369e-01f,-6.0551107e-01f},{5.6066155e-01f,-8.2804507e-01f},{2.6671275e-01f,-9.6377605e-01f},{-5.5195246e-02f,-9.9847555e-01f},
{-3.713172e-01f,-9.285061e-01f},{-6.485144e-01f,-7.612024e-01f},{1e+00f,-0e+00f},{9.466009e-01f,-3.224077e-01f},{7.9210657e-01f,-6.103828e-01f},
{5.530167e-01f,-8.331702e-01f},{2.5486565e-01f,-9.6697646e-01f},{-7.0504576e-02f,-9.9751145e-01f},{-3.8834503e-01f,-9.2151403e-01f},
{-6.64711e-01f,-7.471006e-01f},{1e+00f,-0e+00f},{9.456073e-01f,-3.253103e-01f},{7.883464e-01f,-6.152316e-01f},{5.45325e-01f,-8.382247e-01f},
{2.4298018e-01f,-9.7003126e-01f},{-8.579731e-02f,-9.963126e-01f},{-4.052413e-01f,-9.142098e-01f},{-6.80601e-01f,-7.326543e-01f},{1e+00f,-0e+00f},
{9.446048e-01f,-3.2820985e-01f},{7.8455657e-01f,-6.200572e-01f},{5.3758705e-01f,-8.4320825e-01f},{2.310581e-01f,-9.7293997e-01f},
{-1.0106986e-01f,-9.948793e-01f},{-4.2200026e-01f,-9.065957e-01f},{-6.961771e-01f,-7.1787006e-01f},{1e+00f,-0e+00f},{9.4359344e-01f,-3.311063e-01f},
{7.807372e-01f,-6.248595e-01f},{5.2980363e-01f,-8.4812033e-01f},{2.1910124e-01f,-9.757021e-01f},{-1.1631863e-01f,-9.932119e-01f},
{-4.3861625e-01f,-8.986745e-01f},{-7.114322e-01f,-7.0275474e-01f},{1e+00f,-0e+00f},{9.425732e-01f,-3.3399966e-01f},{7.768885e-01f,-6.2963825e-01f},
{5.219753e-01f,-8.529606e-01f},{2.0711137e-01f,-9.783174e-01f},{-1.3154003e-01f,-9.9131083e-01f},{-4.5508358e-01f,-8.9044875e-01f},
{-7.263591e-01f,-6.8731534e-01f},{1e+00f,-0e+00f},{9.4154406e-01f,-3.3688986e-01f},{7.7301043e-01f,-6.343933e-01f},{5.1410276e-01f,-8.577286e-01f},
{1.9509032e-01f,-9.8078525e-01f},{-1.4673047e-01f,-9.891765e-01f},{-4.7139674e-01f,-8.819213e-01f},{-7.409511e-01f,-6.71559e-01f},{1e+00f,-0e+00f},
{9.4050604e-01f,-3.3977687e-01f},{7.6910335e-01f,-6.3912445e-01f},{5.0618666e-01f,-8.6242396e-01f},{1.8303989e-01f,-9.831055e-01f},
{-1.618864e-01f,-9.868094e-01f},{-4.8755017e-01f,-8.73095e-01f},{-7.552014e-01f,-6.5549284e-01f},{1e+00f,-0e+00f},{9.394592e-01f,-3.4266073e-01f},
{7.6516724e-01f,-6.4383155e-01f},{4.9822766e-01f,-8.6704624e-01f},{1.7096189e-01f,-9.8527765e-01f},{-1.7700422e-01f,-9.842101e-01f},
{-5.0353837e-01f,-8.6397284e-01f},{-7.6910335e-01f,-6.3912445e-01f},{1e+00f,-0e+00f},{9.3840355e-01f,-3.4554133e-01f},{7.612024e-01f,-6.485144e-01f},
{4.9022648e-01f,-8.715951e-01f},{1.5885815e-01f,-9.873014e-01f},{-1.920804e-01f,-9.813792e-01f},{-5.19356e-01f,-8.54558e-01f},
{-7.826506e-01f,-6.2246126e-01f},{1e+00f,-0e+00f},{9.37339e-01f,-3.4841868e-01f},{7.572088e-01f,-6.5317285e-01f},{4.8218378e-01f,-8.760701e-01f},
{1.4673047e-01f,-9.891765e-01f},{-2.0711137e-01f,-9.783174e-01f},{-5.3499764e-01f,-8.448536e-01f},{-7.958369e-01f,-6.0551107e-01f},{1e+00f,-0e+00f},
{9.3626565e-01f,-3.5129276e-01f},{7.531868e-01f,-6.578067e-01f},{4.741002e-01f,-8.804709e-01f},{1.345807e-01f,-9.9090266e-01f},
{-2.2209363e-01f,-9.7502536e-01f},{-5.5045795e-01f,-8.348629e-01f},{-8.0865616e-01f,-5.882816e-01f},{1e+00f,-0e+00f},{9.351835e-01f,-3.5416353e-01f},
{7.491364e-01f,-6.624158e-01f},{4.659765e-01f,-8.847971e-01f},{1.2241068e-01f,-9.9247956e-01f},{-2.370236e-01f,-9.715039e-01f},
{-5.657318e-01f,-8.245893e-01f},{-8.211025e-01f,-5.7078075e-01f},{1e+00f,-0e+00f},{9.340925e-01f,-3.5703096e-01f},{7.4505776e-01f,-6.6699994e-01f},
{4.578133e-01f,-8.8904834e-01f},{1.10222206e-01f,-9.93907e-01f},{-2.518978e-01f,-9.677538e-01f},{-5.8081394e-01f,-8.140363e-01f},
{-8.331702e-01f,-5.530167e-01f},{1e+00f,-0e+00f},{9.329928e-01f,-3.5989505e-01f},{7.409511e-01f,-6.71559e-01f},{4.4961134e-01f,-8.932243e-01f},
{9.801714e-02f,-9.951847e-01f},{-2.6671275e-01f,-9.6377605e-01f},{-5.956993e-01f,-8.032075e-01f},{-8.448536e-01f,-5.3499764e-01f},{1e+00f,-0e+00f},
{9.318843e-01f,-3.6275572e-01f},{7.368166e-01f,-6.760927e-01f},{4.4137126e-01f,-8.9732456e-01f},{8.579731e-02f,-9.963126e-01f},
{-2.8146493e-01f,-9.5957154e-01f},{-6.103828e-01f,-7.9210657e-01f},{-8.5614735e-01f,-5.167318e-01f},{1e+00f,-0e+00f},{9.3076694e-01f,-3.6561298e-01f},
{7.326543e-01f,-6.80601e-01f},{4.3309382e-01f,-9.013488e-01f},{7.356457e-02f,-9.9729043e-01f},{-2.961509e-01f,-9.551412e-01f},
{-6.248595e-01f,-7.807372e-01f},{-8.6704624e-01f,-4.9822766e-01f},{1e+00f,-0e+00f},{9.296409e-01f,-3.6846682e-01f},{7.2846437e-01f,-6.850837e-01f},
{4.2477968e-01f,-9.0529674e-01f},{6.1320737e-02f,-9.981181e-01f},{-3.1076714e-01f,-9.5048606e-01f},{-6.3912445e-01f,-7.6910335e-01f},
{-8.775453e-01f,-4.7949377e-01f},{1e+00f,-0e+00f},{9.285061e-01f,-3.713172e-01f},{7.242471e-01f,-6.8954057e-01f},{4.1642955e-01f,-9.09168e-01f},
{4.9067676e-02f,-9.9879545e-01f},{-3.253103e-01f,-9.456073e-01f},{-6.5317285e-01f,-7.572088e-01f},{-8.8763964e-01f,-4.6053872e-01f},{1e+00f,-0e+00f},
{9.273625e-01f,-3.7416407e-01f},{7.2000253e-01f,-6.9397146e-01f},{4.0804416e-01f,-9.129622e-01f},{3.6807224e-02f,-9.9932235e-01f},
{-3.3977687e-01f,-9.4050604e-01f},{-6.6699994e-01f,-7.4505776e-01f},{-8.9732456e-01f,-4.4137126e-01f},{1e+00f,-0e+00f},
{9.262102e-01f,-3.7700742e-01f},{7.1573085e-01f,-6.9837624e-01f},{3.996242e-01f,-9.166791e-01f},{2.4541229e-02f,-9.996988e-01f},
{-3.5416353e-01f,-9.351835e-01f},{-6.80601e-01f,-7.326543e-01f},{-9.065957e-01f,-4.2200026e-01f},{1e+00f,-0e+00f},{9.2504925e-01f,-3.798472e-01f},
{7.114322e-01f,-7.0275474e-01f},{3.9117038e-01f,-9.203183e-01f},{1.2271538e-02f,-9.999247e-01f},{-3.6846682e-01f,-9.296409e-01f},
{-6.9397146e-01f,-7.2000253e-01f},{-9.154487e-01f,-4.0243465e-01f},{1e+00f,-0e+00f},{9.238795e-01f,-3.8268343e-01f},{7.0710677e-01f,-7.0710677e-01f},
{3.8268343e-01f,-9.238795e-01f},{6.123234e-17f,-1e+00f},{-3.8268343e-01f,-9.238795e-01f},{-7.0710677e-01f,-7.0710677e-01f},
{-9.238795e-01f,-3.8268343e-01f},{1e+00f,-0e+00f},{9.227011e-01f,-3.8551605e-01f},{7.0275474e-01f,-7.114322e-01f},{3.7416407e-01f,-9.273625e-01f},
{-1.2271538e-02f,-9.999247e-01f},{-3.9681e-01f,-9.179008e-01f},{-7.2000253e-01f,-6.9397146e-01f},{-9.318843e-01f,-3.6275572e-01f},{1e+00f,-0e+00f},
{9.2151403e-01f,-3.8834503e-01f},{6.9837624e-01f,-7.1573085e-01f},{3.6561298e-01f,-9.3076694e-01f},{-2.4541229e-02f,-9.996988e-01f},
{-4.1084316e-01f,-9.1170603e-01f},{-7.326543e-01f,-6.80601e-01f},{-9.394592e-01f,-3.4266073e-01f},{1e+00f,-0e+00f},{9.203183e-01f,-3.9117038e-01f},
{6.9397146e-01f,-7.2000253e-01f},{3.5703096e-01f,-9.340925e-01f},{-3.6807224e-02f,-9.9932235e-01f},{-4.2477968e-01f,-9.0529674e-01f},
{-7.4505776e-01f,-6.6699994e-01f},{-9.466009e-01f,-3.224077e-01f},{1e+00f,-0e+00f},{9.191139e-01f,-3.9399204e-01f},{6.8954057e-01f,-7.242471e-01f},
{3.4841868e-01f,-9.37339e-01f},{-4.9067676e-02f,-9.9879545e-01f},{-4.3861625e-01f,-8.986745e-01f},{-7.572088e-01f,-6.5317285e-01f},
{-9.53306e-01f,-3.0200595e-01f},{1e+00f,-0e+00f},{9.179008e-01f,-3.9681e-01f},{6.850837e-01f,-7.2846437e-01f},{3.3977687e-01f,-9.4050604e-01f},
{-6.1320737e-02f,-9.981181e-01f},{-4.5234957e-01f,-8.918407e-01f},{-7.6910335e-01f,-6.3912445e-01f},{-9.5957154e-01f,-2.8146493e-01f},
{1e+00f,-0e+00f},{9.166791e-01f,-3.996242e-01f},{6.80601e-01f,-7.326543e-01f},{3.311063e-01f,-9.4359344e-01f},{-7.356457e-02f,-9.9729043e-01f},
{-4.659765e-01f,-8.847971e-01f},{-7.807372e-01f,-6.248595e-01f},{-9.6539444e-01f,-2.607941e-01f},{1e+00f,-0e+00f},{9.154487e-01f,-4.0243465e-01f},
{6.760927e-01f,-7.368166e-01f},{3.224077e-01f,-9.466009e-01f},{-8.579731e-02f,-9.963126e-01f},{-4.7949377e-01f,-8.775453e-01f},
{-7.9210657e-01f,-6.103828e-01f},{-9.7077215e-01f,-2.4000302e-01f},{1e+00f,-0e+00f},{9.142098e-01f,-4.052413e-01f},{6.71559e-01f,-7.409511e-01f},
{3.1368175e-01f,-9.4952816e-01f},{-9.801714e-02f,-9.951847e-01f},{-4.928982e-01f,-8.7008697e-01f},{-8.032075e-01f,-5.956993e-01f},
{-9.757021e-01f,-2.1910124e-01f},{1e+00f,-0e+00f},{9.129622e-01f,-4.0804416e-01f},{6.6699994e-01f,-7.4505776e-01f},{3.0492923e-01f,-9.52375e-01f},
{-1.10222206e-01f,-9.93907e-01f},{-5.0618666e-01f,-8.6242396e-01f},{-8.140363e-01f,-5.8081394e-01f},{-9.801821e-01f,-1.980984e-01f},{1e+00f,-0e+00f},
{9.1170603e-01f,-4.1084316e-01f},{6.624158e-01f,-7.491364e-01f},{2.961509e-01f,-9.551412e-01f},{-1.2241068e-01f,-9.9247956e-01f},
{-5.19356e-01f,-8.54558e-01f},{-8.245893e-01f,-5.657318e-01f},{-9.842101e-01f,-1.7700422e-01f},{1e+00f,-0e+00f},{9.104413e-01f,-4.1363832e-01f},
{6.578067e-01f,-7.531868e-01f},{2.8734747e-01f,-9.5782644e-01f},{-1.345807e-01f,-9.9090266e-01f},{-5.324031e-01f,-8.464909e-01f},
{-8.348629e-01f,-5.5045795e-01f},{-9.8778415e-01f,-1.558284e-01f},{1e+00f,-0e+00f},{9.09168e-01f,-4.1642955e-01f},{6.5317285e-01f,-7.572088e-01f},
{2.785197e-01f,-9.604305e-01f},{-1.4673047e-01f,-9.891765e-01f},{-5.45325e-01f,-8.382247e-01f},{-8.448536e-01f,-5.3499764e-01f},
{-9.9090266e-01f,-1.345807e-01f},{1e+00f,-0e+00f},{9.078861e-01f,-4.192169e-01f},{6.485144e-01f,-7.612024e-01f},{2.696683e-01f,-9.6295327e-01f},
{-1.5885815e-01f,-9.873014e-01f},{-5.581185e-01f,-8.297612e-01f},{-8.54558e-01f,-5.19356e-01f},{-9.935641e-01f,-1.1327095e-01f},{1e+00f,-0e+00f},
{9.065957e-01f,-4.2200026e-01f},{6.4383155e-01f,-7.6516724e-01f},{2.607941e-01f,-9.6539444e-01f},{-1.7096189e-01f,-9.8527765e-01f},
{-5.7078075e-01f,-8.211025e-01f},{-8.6397284e-01f,-5.0353837e-01f},{-9.957674e-01f,-9.1908954e-02f},{1e+00f,-0e+00f},{9.0529674e-01f,-4.2477968e-01f},
{6.3912445e-01f,-7.6910335e-01f},{2.518978e-01f,-9.677538e-01f},{-1.8303989e-01f,-9.831055e-01f},{-5.8330864e-01f,-8.122506e-01f},
{-8.73095e-01f,-4.8755017e-01f},{-9.9751145e-01f,-7.0504576e-02f},{1e+00f,-0e+00f},{9.039893e-01f,-4.2755508e-01f},{6.343933e-01f,-7.7301043e-01f},
{2.4298018e-01f,-9.7003126e-01f},{-1.9509032e-01f,-9.8078525e-01f},{-5.956993e-01f,-8.032075e-01f},{-8.819213e-01f,-4.7139674e-01f},
{-9.9879545e-01f,-4.9067676e-02f},{1e+00f,-0e+00f},{9.026733e-01f,-4.303265e-01f},{6.2963825e-01f,-7.768885e-01f},{2.3404196e-01f,-9.722265e-01f},
{-2.0711137e-01f,-9.783174e-01f},{-6.079498e-01f,-7.939755e-01f},{-8.9044875e-01f,-4.5508358e-01f},{-9.996188e-01f,-2.7608145e-02f},{1e+00f,-0e+00f},
{9.013488e-01f,-4.3309382e-01f},{6.248595e-01f,-7.807372e-01f},{2.2508392e-01f,-9.7433937e-01f},{-2.1910124e-01f,-9.757021e-01f},
{-6.200572e-01f,-7.8455657e-01f},{-8.986745e-01f,-4.3861625e-01f},{-9.9998116e-01f,-6.1358847e-03f},{1e+00f,-0e+00f},{9.000159e-01f,-4.358571e-01f},
{6.200572e-01f,-7.8455657e-01f},{2.161068e-01f,-9.7636974e-01f},{-2.310581e-01f,-9.7293997e-01f},{-6.3201874e-01f,-7.749531e-01f},
{-9.065957e-01f,-4.2200026e-01f},{-9.9988234e-01f,1.5339206e-02f},{1e+00f,-0e+00f},{8.986745e-01f,-4.3861625e-01f},{6.152316e-01f,-7.883464e-01f},
{2.0711137e-01f,-9.783174e-01f},{-2.4298018e-01f,-9.7003126e-01f},{-6.4383155e-01f,-7.6516724e-01f},{-9.142098e-01f,-4.052413e-01f},
{-9.9932235e-01f,3.6807224e-02f},{1e+00f,-0e+00f},{8.9732456e-01f,-4.4137126e-01f},{6.103828e-01f,-7.9210657e-01f},{1.980984e-01f,-9.801821e-01f},
{-2.5486565e-01f,-9.6697646e-01f},{-6.5549284e-01f,-7.552014e-01f},{-9.2151403e-01f,-3.8834503e-01f},{-9.9830157e-01f,5.8258265e-02f},
{1e+00f,-0e+00f},{8.9596623e-01f,-4.4412214e-01f},{6.0551107e-01f,-7.958369e-01f},{1.8906866e-01f,-9.819639e-01f},{-2.6671275e-01f,-9.6377605e-01f},
{-6.6699994e-01f,-7.4505776e-01f},{-9.285061e-01f,-3.713172e-01f},{-9.968203e-01f,7.968244e-02f},{1e+00f,-0e+00f},{8.945995e-01f,-4.4686884e-01f},
{6.0061646e-01f,-7.9953724e-01f},{1.800229e-01f,-9.836624e-01f},{-2.785197e-01f,-9.604305e-01f},{-6.7835003e-01f,-7.347389e-01f},
{-9.351835e-01f,-3.5416353e-01f},{-9.948793e-01f,1.0106986e-01f},{1e+00f,-0e+00f},{8.932243e-01f,-4.4961134e-01f},{5.956993e-01f,-8.032075e-01f},
{1.7096189e-01f,-9.8527765e-01f},{-2.9028466e-01f,-9.5694035e-01f},{-6.8954057e-01f,-7.242471e-01f},{-9.4154406e-01f,-3.3688986e-01f},
{-9.9247956e-01f,1.2241068e-01f},{1e+00f,-0e+00f},{8.918407e-01f,-4.5234957e-01f},{5.907597e-01f,-8.068476e-01f},{1.618864e-01f,-9.868094e-01f},
{-3.0200595e-01f,-9.53306e-01f},{-7.005688e-01f,-7.1358484e-01f},{-9.475856e-01f,-3.1950203e-01f},{-9.89622e-01f,1.4369503e-01f},{1e+00f,-0e+00f},
{8.9044875e-01f,-4.5508358e-01f},{5.8579785e-01f,-8.1045717e-01f},{1.5279719e-01f,-9.882576e-01f},{-3.1368175e-01f,-9.4952816e-01f},
{-7.114322e-01f,-7.0275474e-01f},{-9.53306e-01f,-3.0200595e-01f},{-9.863081e-01f,1.6491312e-01f},{1e+00f,-0e+00f},{8.8904834e-01f,-4.578133e-01f},
{5.8081394e-01f,-8.140363e-01f},{1.4369503e-01f,-9.89622e-01f},{-3.253103e-01f,-9.456073e-01f},{-7.221282e-01f,-6.917592e-01f},
{-9.5870346e-01f,-2.8440753e-01f},{-9.825393e-01f,1.8605515e-01f},{1e+00f,-0e+00f},{8.8763964e-01f,-4.6053872e-01f},{5.7580817e-01f,-8.175848e-01f},
{1.345807e-01f,-9.9090266e-01f},{-3.3688986e-01f,-9.4154406e-01f},{-7.326543e-01f,-6.80601e-01f},{-9.6377605e-01f,-2.6671275e-01f},
{-9.783174e-01f,2.0711137e-01f},{1e+00f,-0e+00f},{8.8622254e-01f,-4.632598e-01f},{5.7078075e-01f,-8.211025e-01f},{1.2545498e-01f,-9.920993e-01f},
{-3.4841868e-01f,-9.37339e-01f},{-7.4300796e-01f,-6.692826e-01f},{-9.685221e-01f,-2.4892761e-01f},{-9.7364426e-01f,2.2807208e-01f},{1e+00f,-0e+00f},
{8.847971e-01f,-4.659765e-01f},{5.657318e-01f,-8.245893e-01f},{1.1631863e-01f,-9.932119e-01f},{-3.5989505e-01f,-9.329928e-01f},
{-7.531868e-01f,-6.578067e-01f},{-9.7293997e-01f,-2.310581e-01f},{-9.685221e-01f,2.4892761e-01f},{1e+00f,-0e+00f},{8.8336337e-01f,-4.6868882e-01f},
{5.6066155e-01f,-8.2804507e-01f},{1.0717242e-01f,-9.9424046e-01f},{-3.713172e-01f,-9.285061e-01f},{-7.631884e-01f,-6.4617604e-01f},
{-9.7702813e-01f,-2.1311031e-01f},{-9.6295327e-01f,2.696683e-01f},{1e+00f,-0e+00f},{8.819213e-01f,-4.7139674e-01f},{5.5557024e-01f,-8.314696e-01f},
{9.801714e-02f,-9.951847e-01f},{-3.8268343e-01f,-9.238795e-01f},{-7.7301043e-01f,-6.343933e-01f},{-9.8078525e-01f,-1.9509032e-01f},
{-9.5694035e-01f,2.9028466e-01f},{1e+00f,-0e+00f},{8.804709e-01f,-4.741002e-01f},{5.5045795e-01f,-8.348629e-01f},{8.885355e-02f,-9.960447e-01f},
{-3.9399204e-01f,-9.191139e-01f},{-7.826506e-01f,-6.2246126e-01f},{-9.842101e-01f,-1.7700422e-01f},{-9.5048606e-01f,3.1076714e-01f},{1e+00f,-0e+00f},
{8.790122e-01f,-4.7679922e-01f},{5.45325e-01f,-8.382247e-01f},{7.968244e-02f,-9.968203e-01f},{-4.052413e-01f,-9.142098e-01f},
{-7.9210657e-01f,-6.103828e-01f},{-9.873014e-01f,-1.5885815e-01f},{-9.4359344e-01f,3.311063e-01f},{1e+00f,-0e+00f},{8.775453e-01f,-4.7949377e-01f},
{5.4017144e-01f,-8.41555e-01f},{7.0504576e-02f,-9.9751145e-01f},{-4.1642955e-01f,-9.09168e-01f},{-8.0137616e-01f,-5.981607e-01f},
{-9.900582e-01f,-1.4065824e-01f},{-9.3626565e-01f,3.5129276e-01f},{1e+00f,-0e+00f},{8.760701e-01f,-4.8218378e-01f},{5.3499764e-01f,-8.448536e-01f},
{6.1320737e-02f,-9.981181e-01f},{-4.2755508e-01f,-9.039893e-01f},{-8.1045717e-01f,-5.8579785e-01f},{-9.9247956e-01f,-1.2241068e-01f},
{-9.285061e-01f,3.713172e-01f},{1e+00f,-0e+00f},{8.7458664e-01f,-4.8486924e-01f},{5.2980363e-01f,-8.4812033e-01f},{5.2131705e-02f,-9.9864024e-01f},
{-4.3861625e-01f,-8.986745e-01f},{-8.193475e-01f,-5.7329714e-01f},{-9.945646e-01f,-1.0412163e-01f},{-9.203183e-01f,3.9117038e-01f},{1e+00f,-0e+00f},
{8.73095e-01f,-4.8755017e-01f},{5.2458966e-01f,-8.513552e-01f},{4.293826e-02f,-9.9907774e-01f},{-4.4961134e-01f,-8.932243e-01f},
{-8.2804507e-01f,-5.6066155e-01f},{-9.963126e-01f,-8.579731e-02f},{-9.1170603e-01f,4.1084316e-01f},{1e+00f,-0e+00f},{8.715951e-01f,-4.9022648e-01f},
{5.19356e-01f,-8.54558e-01f},{3.3741172e-02f,-9.994306e-01f},{-4.6053872e-01f,-8.8763964e-01f},{-8.3654773e-01f,-5.4789406e-01f},
{-9.9772304e-01f,-6.744392e-02f},{-9.026733e-01f,4.303265e-01f},{1e+00f,-0e+00f},{8.7008697e-01f,-4.928982e-01f},{5.1410276e-01f,-8.577286e-01f},
{2.4541229e-02f,-9.996988e-01f},{-4.7139674e-01f,-8.819213e-01f},{-8.448536e-01f,-5.3499764e-01f},{-9.9879545e-01f,-4.9067676e-02f},
{-8.932243e-01f,4.4961134e-01f},{1e+00f,-0e+00f},{8.685707e-01f,-4.9556527e-01f},{5.0883013e-01f,-8.6086696e-01f},{1.5339206e-02f,-9.9988234e-01f},
{-4.8218378e-01f,-8.760701e-01f},{-8.529606e-01f,-5.219753e-01f},{-9.995294e-01f,-3.0674804e-02f},{-8.8336337e-01f,4.6868882e-01f},{1e+00f,-0e+00f},
{8.6704624e-01f,-4.9822766e-01f},{5.0353837e-01f,-8.6397284e-01f},{6.1358847e-03f,-9.9998116e-01f},{-4.928982e-01f,-8.7008697e-01f},
{-8.6086696e-01f,-5.0883013e-01f},{-9.999247e-01f,-1.2271538e-02f},{-8.73095e-01f,4.8755017e-01f},{1e+00f,-0e+00f},{8.655136e-01f,-5.0088537e-01f},
{4.9822766e-01f,-8.6704624e-01f},{-3.0679568e-03f,-9.999953e-01f},{-5.0353837e-01f,-8.6397284e-01f},{-8.685707e-01f,-4.9556527e-01f},
{-9.9998116e-01f,6.1358847e-03f},{-8.6242396e-01f,5.0618666e-01f},{1e+00f,-0e+00f},{8.6397284e-01f,-5.0353837e-01f},{4.928982e-01f,-8.7008697e-01f},
{-1.2271538e-02f,-9.999247e-01f},{-5.1410276e-01f,-8.577286e-01f},{-8.760701e-01f,-4.8218378e-01f},{-9.996988e-01f,2.4541229e-02f},
{-8.513552e-01f,5.2458966e-01f},{1e+00f,-0e+00f},{8.6242396e-01f,-5.0618666e-01f},{4.8755017e-01f,-8.73095e-01f},{-2.147408e-02f,-9.997694e-01f},
{-5.2458966e-01f,-8.513552e-01f},{-8.8336337e-01f,-4.6868882e-01f},{-9.9907774e-01f,4.293826e-02f},{-8.398938e-01f,5.427508e-01f},{1e+00f,-0e+00f},
{8.6086696e-01f,-5.0883013e-01f},{4.8218378e-01f,-8.760701e-01f},{-3.0674804e-02f,-9.995294e-01f},{-5.3499764e-01f,-8.448536e-01f},
{-8.9044875e-01f,-4.5508358e-01f},{-9.981181e-01f,6.1320737e-02f},{-8.2804507e-01f,5.6066155e-01f},{1e+00f,-0e+00f},{8.593018e-01f,-5.114688e-01f},
{4.7679922e-01f,-8.790122e-01f},{-3.9872926e-02f,-9.9920475e-01f},{-5.45325e-01f,-8.382247e-01f},{-8.9732456e-01f,-4.4137126e-01f},
{-9.968203e-01f,7.968244e-02f},{-8.1581444e-01f,5.7831377e-01f},{1e+00f,-0e+00f},{8.577286e-01f,-5.1410276e-01f},{4.7139674e-01f,-8.819213e-01f},
{-4.9067676e-02f,-9.9879545e-01f},{-5.5557024e-01f,-8.314696e-01f},{-9.039893e-01f,-4.2755508e-01f},{-9.951847e-01f,9.801714e-02f},
{-8.032075e-01f,5.956993e-01f},{1e+00f,-0e+00f},{8.5614735e-01f,-5.167318e-01f},{4.659765e-01f,-8.847971e-01f},{-5.8258265e-02f,-9.9830157e-01f},
{-5.657318e-01f,-8.245893e-01f},{-9.104413e-01f,-4.1363832e-01f},{-9.932119e-01f,1.1631863e-01f},{-7.902302e-01f,6.128101e-01f},{1e+00f,-0e+00f},
{8.54558e-01f,-5.19356e-01f},{4.6053872e-01f,-8.8763964e-01f},{-6.744392e-02f,-9.9772304e-01f},{-5.7580817e-01f,-8.175848e-01f},
{-9.166791e-01f,-3.996242e-01f},{-9.9090266e-01f,1.345807e-01f},{-7.768885e-01f,6.2963825e-01f},{1e+00f,-0e+00f},{8.529606e-01f,-5.219753e-01f},
{4.5508358e-01f,-8.9044875e-01f},{-7.6623864e-02f,-9.9706006e-01f},{-5.8579785e-01f,-8.1045717e-01f},{-9.227011e-01f,-3.8551605e-01f},
{-9.882576e-01f,1.5279719e-01f},{-7.631884e-01f,6.4617604e-01f},{1e+00f,-0e+00f},{8.513552e-01f,-5.2458966e-01f},{4.4961134e-01f,-8.932243e-01f},
{-8.579731e-02f,-9.963126e-01f},{-5.956993e-01f,-8.032075e-01f},{-9.285061e-01f,-3.713172e-01f},{-9.8527765e-01f,1.7096189e-01f},
{-7.491364e-01f,6.624158e-01f},{1e+00f,-0e+00f},{8.4974176e-01f,-5.2719915e-01f},{4.4412214e-01f,-8.9596623e-01f},{-9.49635e-02f,-9.954808e-01f},
{-6.0551107e-01f,-7.958369e-01f},{-9.340925e-01f,-3.5703096e-01f},{-9.819639e-01f,1.8906866e-01f},{-7.347389e-01f,6.7835003e-01f},{1e+00f,-0e+00f},
{8.4812033e-01f,-5.2980363e-01f},{4.3861625e-01f,-8.986745e-01f},{-1.0412163e-01f,-9.945646e-01f},{-6.152316e-01f,-7.883464e-01f},
{-9.394592e-01f,-3.4266073e-01f},{-9.783174e-01f,2.0711137e-01f},{-7.2000253e-01f,6.9397146e-01f},{1e+00f,-0e+00f},{8.464909e-01f,-5.324031e-01f},
{4.3309382e-01f,-9.013488e-01f},{-1.1327095e-01f,-9.935641e-01f},{-6.248595e-01f,-7.807372e-01f},{-9.446048e-01f,-3.2820985e-01f},
{-9.7433937e-01f,2.2508392e-01f},{-7.0493406e-01f,7.092728e-01f},{1e+00f,-0e+00f},{8.448536e-01f,-5.3499764e-01f},{4.2755508e-01f,-9.039893e-01f},
{-1.2241068e-01f,-9.9247956e-01f},{-6.343933e-01f,-7.7301043e-01f},{-9.4952816e-01f,-3.1368175e-01f},{-9.7003126e-01f,2.4298018e-01f},
{-6.8954057e-01f,7.242471e-01f},{1e+00f,-0e+00f},{8.4320825e-01f,-5.3758705e-01f},{4.2200026e-01f,-9.065957e-01f},{-1.3154003e-01f,-9.9131083e-01f},
{-6.4383155e-01f,-7.6516724e-01f},{-9.542281e-01f,-2.9907984e-01f},{-9.6539444e-01f,2.607941e-01f},{-6.73829e-01f,7.388873e-01f},{1e+00f,-0e+00f},
{8.41555e-01f,-5.4017144e-01f},{4.1642955e-01f,-9.09168e-01f},{-1.4065824e-01f,-9.900582e-01f},{-6.5317285e-01f,-7.572088e-01f},
{-9.5870346e-01f,-2.8440753e-01f},{-9.604305e-01f,2.785197e-01f},{-6.578067e-01f,7.531868e-01f},{1e+00f,-0e+00f},{8.398938e-01f,-5.427508e-01f},
{4.1084316e-01f,-9.1170603e-01f},{-1.4976454e-01f,-9.8872167e-01f},{-6.624158e-01f,-7.491364e-01f},{-9.6295327e-01f,-2.696683e-01f},
{-9.551412e-01f,2.961509e-01f},{-6.4148104e-01f,7.671389e-01f},{1e+00f,-0e+00f},{8.382247e-01f,-5.45325e-01f},{4.052413e-01f,-9.142098e-01f},
{-1.5885815e-01f,-9.873014e-01f},{-6.71559e-01f,-7.409511e-01f},{-9.6697646e-01f,-2.5486565e-01f},{-9.4952816e-01f,3.1368175e-01f},
{-6.248595e-01f,7.807372e-01f},{1e+00f,-0e+00f},{8.3654773e-01f,-5.4789406e-01f},{3.996242e-01f,-9.166791e-01f},{-1.6793829e-01f,-9.857975e-01f},
{-6.80601e-01f,-7.326543e-01f},{-9.7077215e-01f,-2.4000302e-01f},{-9.4359344e-01f,3.311063e-01f},{-6.079498e-01f,7.939755e-01f},{1e+00f,-0e+00f},
{8.348629e-01f,-5.5045795e-01f},{3.9399204e-01f,-9.191139e-01f},{-1.7700422e-01f,-9.842101e-01f},{-6.8954057e-01f,-7.242471e-01f},
{-9.7433937e-01f,-2.2508392e-01f},{-9.37339e-01f,3.4841868e-01f},{-5.907597e-01f,8.068476e-01f},{1e+00f,-0e+00f},{8.331702e-01f,-5.530167e-01f},
{3.8834503e-01f,-9.2151403e-01f},{-1.8605515e-01f,-9.825393e-01f},{-6.9837624e-01f,-7.1573085e-01f},{-9.7767735e-01f,-2.1011184e-01f},
{-9.3076694e-01f,3.6561298e-01f},{-5.7329714e-01f,8.193475e-01f},{1e+00f,-0e+00f},{8.314696e-01f,-5.5557024e-01f},{3.8268343e-01f,-9.238795e-01f},
{-1.9509032e-01f,-9.8078525e-01f},{-7.0710677e-01f,-7.0710677e-01f},{-9.8078525e-01f,-1.9509032e-01f},{-9.238795e-01f,3.8268343e-01f},
{-5.5557024e-01f,8.314696e-01f},{1e+00f,-0e+00f},{8.297612e-01f,-5.581185e-01f},{3.7700742e-01f,-9.262102e-01f},{-2.0410897e-01f,-9.789482e-01f},
{-7.1573085e-01f,-6.9837624e-01f},{-9.836624e-01f,-1.800229e-01f},{-9.166791e-01f,3.996242e-01f},{-5.3758705e-01f,8.4320825e-01f},{1e+00f,-0e+00f},
{8.2804507e-01f,-5.6066155e-01f},{3.713172e-01f,-9.285061e-01f},{-2.1311031e-01f,-9.7702813e-01f},{-7.242471e-01f,-6.8954057e-01f},
{-9.863081e-01f,-1.6491312e-01f},{-9.09168e-01f,4.1642955e-01f},{-5.19356e-01f,8.54558e-01f},{1e+00f,-0e+00f},{8.2632107e-01f,-5.6319934e-01f},
{3.6561298e-01f,-9.3076694e-01f},{-2.2209363e-01f,-9.7502536e-01f},{-7.326543e-01f,-6.80601e-01f},{-9.8872167e-01f,-1.4976454e-01f},
{-9.013488e-01f,4.3309382e-01f},{-5.0088537e-01f,8.655136e-01f},{1e+00f,-0e+00f},{8.245893e-01f,-5.657318e-01f},{3.5989505e-01f,-9.329928e-01f},
{-2.310581e-01f,-9.7293997e-01f},{-7.409511e-01f,-6.71559e-01f},{-9.9090266e-01f,-1.345807e-01f},{-8.932243e-01f,4.4961134e-01f},
{-4.8218378e-01f,8.760701e-01f},{1e+00f,-0e+00f},{8.228498e-01f,-5.6825894e-01f},{3.5416353e-01f,-9.351835e-01f},{-2.4000302e-01f,-9.7077215e-01f},
{-7.491364e-01f,-6.624158e-01f},{-9.928504e-01f,-1.19365215e-01f},{-8.847971e-01f,4.659765e-01f},{-4.632598e-01f,8.8622254e-01f},{1e+00f,-0e+00f},
{8.211025e-01f,-5.7078075e-01f},{3.4841868e-01f,-9.37339e-01f},{-2.4892761e-01f,-9.685221e-01f},{-7.572088e-01f,-6.5317285e-01f},
{-9.945646e-01f,-1.0412163e-01f},{-8.760701e-01f,4.8218378e-01f},{-4.4412214e-01f,8.9596623e-01f},{1e+00f,-0e+00f},{8.193475e-01f,-5.7329714e-01f},
{3.4266073e-01f,-9.394592e-01f},{-2.578311e-01f,-9.6619e-01f},{-7.6516724e-01f,-6.4383155e-01f},{-9.960447e-01f,-8.885355e-02f},
{-8.6704624e-01f,4.9822766e-01f},{-4.2477968e-01f,9.0529674e-01f},{1e+00f,-0e+00f},{8.175848e-01f,-5.7580817e-01f},{3.3688986e-01f,-9.4154406e-01f},
{-2.6671275e-01f,-9.6377605e-01f},{-7.7301043e-01f,-6.343933e-01f},{-9.9729043e-01f,-7.356457e-02f},{-8.577286e-01f,5.1410276e-01f},
{-4.052413e-01f,9.142098e-01f},{1e+00f,-0e+00f},{8.1581444e-01f,-5.7831377e-01f},{3.311063e-01f,-9.4359344e-01f},{-2.7557182e-01f,-9.6128047e-01f},
{-7.807372e-01f,-6.248595e-01f},{-9.9830157e-01f,-5.8258265e-02f},{-8.4812033e-01f,5.2980363e-01f},{-3.8551605e-01f,9.227011e-01f},{1e+00f,-0e+00f},
{8.140363e-01f,-5.8081394e-01f},{3.253103e-01f,-9.456073e-01f},{-2.8440753e-01f,-9.5870346e-01f},{-7.883464e-01f,-6.152316e-01f},
{-9.9907774e-01f,-4.293826e-02f},{-8.382247e-01f,5.45325e-01f},{-3.6561298e-01f,9.3076694e-01f},{1e+00f,-0e+00f},{8.122506e-01f,-5.8330864e-01f},
{3.1950203e-01f,-9.475856e-01f},{-2.9321915e-01f,-9.5604527e-01f},{-7.958369e-01f,-6.0551107e-01f},{-9.996188e-01f,-2.7608145e-02f},
{-8.2804507e-01f,5.6066155e-01f},{-3.4554133e-01f,9.3840355e-01f},{1e+00f,-0e+00f},{8.1045717e-01f,-5.8579785e-01f},{3.1368175e-01f,-9.4952816e-01f},
{-3.0200595e-01f,-9.53306e-01f},{-8.032075e-01f,-5.956993e-01f},{-9.999247e-01f,-1.2271538e-02f},{-8.175848e-01f,5.7580817e-01f},
{-3.253103e-01f,9.456073e-01f},{1e+00f,-0e+00f},{8.0865616e-01f,-5.882816e-01f},{3.0784965e-01f,-9.51435e-01f},{-3.1076714e-01f,-9.5048606e-01f},
{-8.1045717e-01f,-5.8579785e-01f},{-9.999953e-01f,3.0679568e-03f},{-8.068476e-01f,5.907597e-01f},{-3.0492923e-01f,9.52375e-01f},{1e+00f,-0e+00f},
{8.068476e-01f,-5.907597e-01f},{3.0200595e-01f,-9.53306e-01f},{-3.1950203e-01f,-9.475856e-01f},{-8.175848e-01f,-5.7580817e-01f},
{-9.998306e-01f,1.840673e-02f},{-7.958369e-01f,6.0551107e-01f},{-2.8440753e-01f,9.5870346e-01f},{1e+00f,-0e+00f},{8.0503136e-01f,-5.932323e-01f},
{2.961509e-01f,-9.551412e-01f},{-3.2820985e-01f,-9.446048e-01f},{-8.245893e-01f,-5.657318e-01f},{-9.994306e-01f,3.3741172e-02f},
{-7.8455657e-01f,6.200572e-01f},{-2.6375467e-01f,9.645898e-01f},{1e+00f,-0e+00f},{8.032075e-01f,-5.956993e-01f},{2.9028466e-01f,-9.5694035e-01f},
{-3.3688986e-01f,-9.4154406e-01f},{-8.314696e-01f,-5.5557024e-01f},{-9.9879545e-01f,4.9067676e-02f},{-7.7301043e-01f,6.343933e-01f},
{-2.4298018e-01f,9.7003126e-01f},{1e+00f,-0e+00f},{8.0137616e-01f,-5.981607e-01f},{2.8440753e-01f,-9.5870346e-01f},{-3.4554133e-01f,-9.3840355e-01f},
{-8.382247e-01f,-5.45325e-01f},{-9.979253e-01f,6.438263e-02f},{-7.612024e-01f,6.485144e-01f},{-2.2209363e-01f,9.7502536e-01f},{1e+00f,-0e+00f},
{7.9953724e-01f,-6.0061646e-01f},{2.785197e-01f,-9.604305e-01f},{-3.5416353e-01f,-9.351835e-01f},{-8.448536e-01f,-5.3499764e-01f},
{-9.968203e-01f,7.968244e-02f},{-7.491364e-01f,6.624158e-01f},{-2.0110464e-01f,9.795698e-01f},{1e+00f,-0e+00f},{7.9769087e-01f,-6.030666e-01f},
{2.7262136e-01f,-9.621214e-01f},{-3.6275572e-01f,-9.318843e-01f},{-8.513552e-01f,-5.2458966e-01f},{-9.954808e-01f,9.49635e-02f},
{-7.368166e-01f,6.760927e-01f},{-1.800229e-01f,9.836624e-01f},{1e+00f,-0e+00f},{7.958369e-01f,-6.0551107e-01f},{2.6671275e-01f,-9.6377605e-01f},
{-3.713172e-01f,-9.285061e-01f},{-8.577286e-01f,-5.1410276e-01f},{-9.93907e-01f,1.10222206e-01f},{-7.242471e-01f,6.8954057e-01f},
{-1.5885815e-01f,9.873014e-01f},{1e+00f,-0e+00f},{7.939755e-01f,-6.079498e-01f},{2.607941e-01f,-9.6539444e-01f},{-3.798472e-01f,-9.2504925e-01f},
{-8.6397284e-01f,-5.0353837e-01f},{-9.920993e-01f,1.2545498e-01f},{-7.114322e-01f,7.0275474e-01f},{-1.3762012e-01f,9.904851e-01f},{1e+00f,-0e+00f},
{7.9210657e-01f,-6.103828e-01f},{2.5486565e-01f,-9.6697646e-01f},{-3.8834503e-01f,-9.2151403e-01f},{-8.7008697e-01f,-4.928982e-01f},
{-9.900582e-01f,1.4065824e-01f},{-6.9837624e-01f,7.1573085e-01f},{-1.1631863e-01f,9.932119e-01f},{1e+00f,-0e+00f},{7.902302e-01f,-6.128101e-01f},
{2.4892761e-01f,-9.685221e-01f},{-3.9681e-01f,-9.179008e-01f},{-8.760701e-01f,-4.8218378e-01f},{-9.8778415e-01f,1.558284e-01f},
{-6.850837e-01f,7.2846437e-01f},{-9.49635e-02f,9.954808e-01f},{1e+00f,-0e+00f},{7.883464e-01f,-6.152316e-01f},{2.4298018e-01f,-9.7003126e-01f},
{-4.052413e-01f,-9.142098e-01f},{-8.819213e-01f,-4.7139674e-01f},{-9.8527765e-01f,1.7096189e-01f},{-6.71559e-01f,7.409511e-01f},
{-7.356457e-02f,9.9729043e-01f},{1e+00f,-0e+00f},{7.864552e-01f,-6.176473e-01f},{2.370236e-01f,-9.715039e-01f},{-4.1363832e-01f,-9.104413e-01f},
{-8.8763964e-01f,-4.6053872e-01f},{-9.825393e-01f,1.8605515e-01f},{-6.578067e-01f,7.531868e-01f},{-5.2131705e-02f,9.9864024e-01f},{1e+00f,-0e+00f},
{7.8455657e-01f,-6.200572e-01f},{2.310581e-01f,-9.7293997e-01f},{-4.2200026e-01f,-9.065957e-01f},{-8.932243e-01f,-4.4961134e-01f},
{-9.795698e-01f,2.0110464e-01f},{-6.4383155e-01f,7.6516724e-01f},{-3.0674804e-02f,9.995294e-01f},{1e+00f,-0e+00f},{7.826506e-01f,-6.2246126e-01f},
{2.2508392e-01f,-9.7433937e-01f},{-4.303265e-01f,-9.026733e-01f},{-8.986745e-01f,-4.3861625e-01f},{-9.7636974e-01f,2.161068e-01f},
{-6.2963825e-01f,7.768885e-01f},{-9.203754e-03f,9.999576e-01f},{1e+00f,-0e+00f},{7.807372e-01f,-6.248595e-01f},{2.1910124e-01f,-9.757021e-01f},
{-4.3861625e-01f,-8.986745e-01f},{-9.039893e-01f,-4.2755508e-01f},{-9.7293997e-01f,2.310581e-01f},{-6.152316e-01f,7.883464e-01f},
{1.2271538e-02f,9.999247e-01f},{1e+00f,-0e+00f},{7.788165e-01f,-6.272518e-01f},{2.1311031e-01f,-9.7702813e-01f},{-4.4686884e-01f,-8.945995e-01f},
{-9.09168e-01f,-4.1642955e-01f},{-9.6928126e-01f,2.4595505e-01f},{-6.0061646e-01f,7.9953724e-01f},{3.3741172e-02f,9.994306e-01f},{1e+00f,-0e+00f},
{7.768885e-01f,-6.2963825e-01f},{2.0711137e-01f,-9.783174e-01f},{-4.5508358e-01f,-8.9044875e-01f},{-9.142098e-01f,-4.052413e-01f},
{-9.6539444e-01f,2.607941e-01f},{-5.8579785e-01f,8.1045717e-01f},{5.5195246e-02f,9.9847555e-01f},{1e+00f,-0e+00f},{7.749531e-01f,-6.3201874e-01f},
{2.0110464e-01f,-9.795698e-01f},{-4.632598e-01f,-8.8622254e-01f},{-9.191139e-01f,-3.9399204e-01f},{-9.6128047e-01f,2.7557182e-01f},
{-5.7078075e-01f,8.211025e-01f},{7.6623864e-02f,9.9706006e-01f},{1e+00f,-0e+00f},{7.7301043e-01f,-6.343933e-01f},{1.9509032e-01f,-9.8078525e-01f},
{-4.7139674e-01f,-8.819213e-01f},{-9.238795e-01f,-3.8268343e-01f},{-9.5694035e-01f,2.9028466e-01f},{-5.5557024e-01f,8.314696e-01f},
{9.801714e-02f,9.951847e-01f},{1e+00f,-0e+00f},{7.710605e-01f,-6.3676184e-01f},{1.8906866e-01f,-9.819639e-01f},{-4.7949377e-01f,-8.775453e-01f},
{-9.285061e-01f,-3.713172e-01f},{-9.52375e-01f,3.0492923e-01f},{-5.4017144e-01f,8.41555e-01f},{1.19365215e-01f,9.928504e-01f},{1e+00f,-0e+00f},
{7.6910335e-01f,-6.3912445e-01f},{1.8303989e-01f,-9.831055e-01f},{-4.8755017e-01f,-8.73095e-01f},{-9.329928e-01f,-3.5989505e-01f},
{-9.475856e-01f,3.1950203e-01f},{-5.2458966e-01f,8.513552e-01f},{1.4065824e-01f,9.900582e-01f},{1e+00f,-0e+00f},{7.671389e-01f,-6.4148104e-01f},
{1.7700422e-01f,-9.842101e-01f},{-4.9556527e-01f,-8.685707e-01f},{-9.37339e-01f,-3.4841868e-01f},{-9.425732e-01f,3.3399966e-01f},
{-5.0883013e-01f,8.6086696e-01f},{1.618864e-01f,9.868094e-01f},{1e+00f,-0e+00f},{7.6516724e-01f,-6.4383155e-01f},{1.7096189e-01f,-9.8527765e-01f},
{-5.0353837e-01f,-8.6397284e-01f},{-9.4154406e-01f,-3.3688986e-01f},{-9.37339e-01f,3.4841868e-01f},{-4.928982e-01f,8.7008697e-01f},
{1.8303989e-01f,9.831055e-01f},{1e+00f,-0e+00f},{7.631884e-01f,-6.4617604e-01f},{1.6491312e-01f,-9.863081e-01f},{-5.114688e-01f,-8.593018e-01f},
{-9.456073e-01f,-3.253103e-01f},{-9.318843e-01f,3.6275572e-01f},{-4.7679922e-01f,8.790122e-01f},{2.0410897e-01f,9.789482e-01f},{1e+00f,-0e+00f},
{7.612024e-01f,-6.485144e-01f},{1.5885815e-01f,-9.873014e-01f},{-5.19356e-01f,-8.54558e-01f},{-9.4952816e-01f,-3.1368175e-01f},
{-9.262102e-01f,3.7700742e-01f},{-4.6053872e-01f,8.8763964e-01f},{2.2508392e-01f,9.7433937e-01f},{1e+00f,-0e+00f},{7.592092e-01f,-6.5084666e-01f},
{1.5279719e-01f,-9.882576e-01f},{-5.2719915e-01f,-8.4974176e-01f},{-9.53306e-01f,-3.0200595e-01f},{-9.203183e-01f,3.9117038e-01f},
{-4.4412214e-01f,8.9596623e-01f},{2.4595505e-01f,9.6928126e-01f},{1e+00f,-0e+00f},{7.572088e-01f,-6.5317285e-01f},{1.4673047e-01f,-9.891765e-01f},
{-5.3499764e-01f,-8.448536e-01f},{-9.5694035e-01f,-2.9028466e-01f},{-9.142098e-01f,4.052413e-01f},{-4.2755508e-01f,9.039893e-01f},
{2.6671275e-01f,9.6377605e-01f},{1e+00f,-0e+00f},{7.552014e-01f,-6.5549284e-01f},{1.4065824e-01f,-9.900582e-01f},{-5.427508e-01f,-8.398938e-01f},
{-9.604305e-01f,-2.785197e-01f},{-9.078861e-01f,4.192169e-01f},{-4.1084316e-01f,9.1170603e-01f},{2.8734747e-01f,9.5782644e-01f},{1e+00f,-0e+00f},
{7.531868e-01f,-6.578067e-01f},{1.345807e-01f,-9.9090266e-01f},{-5.5045795e-01f,-8.348629e-01f},{-9.6377605e-01f,-2.6671275e-01f},
{-9.013488e-01f,4.3309382e-01f},{-3.9399204e-01f,9.191139e-01f},{3.0784965e-01f,9.51435e-01f},{1e+00f,-0e+00f},{7.5116515e-01f,-6.6011435e-01f},
{1.284981e-01f,-9.9170977e-01f},{-5.581185e-01f,-8.297612e-01f},{-9.6697646e-01f,-2.5486565e-01f},{-8.945995e-01f,4.4686884e-01f},
{-3.7700742e-01f,9.262102e-01f},{3.2820985e-01f,9.446048e-01f},{1e+00f,-0e+00f},{7.491364e-01f,-6.624158e-01f},{1.2241068e-01f,-9.9247956e-01f},
{-5.657318e-01f,-8.245893e-01f},{-9.7003126e-01f,-2.4298018e-01f},{-8.8763964e-01f,4.6053872e-01f},{-3.5989505e-01f,9.329928e-01f},
{3.4841868e-01f,9.37339e-01f},{1e+00f,-0e+00f},{7.471006e-01f,-6.64711e-01f},{1.1631863e-01f,-9.932119e-01f},{-5.7329714e-01f,-8.193475e-01f},
{-9.7293997e-01f,-2.310581e-01f},{-8.804709e-01f,4.741002e-01f},{-3.4266073e-01f,9.394592e-01f},{3.6846682e-01f,9.296409e-01f},{1e+00f,-0e+00f},
{7.4505776e-01f,-6.6699994e-01f},{1.10222206e-01f,-9.93907e-01f},{-5.8081394e-01f,-8.140363e-01f},{-9.757021e-01f,-2.1910124e-01f},
{-8.73095e-01f,4.8755017e-01f},{-3.253103e-01f,9.456073e-01f},{3.8834503e-01f,9.2151403e-01f},{1e+00f,-0e+00f},{7.4300796e-01f,-6.692826e-01f},
{1.0412163e-01f,-9.945646e-01f},{-5.882816e-01f,-8.0865616e-01f},{-9.783174e-01f,-2.0711137e-01f},{-8.655136e-01f,5.0088537e-01f},
{-3.0784965e-01f,9.51435e-01f},{4.0804416e-01f,9.129622e-01f},{1e+00f,-0e+00f},{7.409511e-01f,-6.71559e-01f},{9.801714e-02f,-9.951847e-01f},
{-5.956993e-01f,-8.032075e-01f},{-9.8078525e-01f,-1.9509032e-01f},{-8.577286e-01f,5.1410276e-01f},{-2.9028466e-01f,9.5694035e-01f},
{4.2755508e-01f,9.039893e-01f},{1e+00f,-0e+00f},{7.388873e-01f,-6.73829e-01f},{9.1908954e-02f,-9.957674e-01f},{-6.030666e-01f,-7.9769087e-01f},
{-9.831055e-01f,-1.8303989e-01f},{-8.4974176e-01f,5.2719915e-01f},{-2.7262136e-01f,9.621214e-01f},{4.4686884e-01f,8.945995e-01f},{1e+00f,-0e+00f},
{7.368166e-01f,-6.760927e-01f},{8.579731e-02f,-9.963126e-01f},{-6.103828e-01f,-7.9210657e-01f},{-9.8527765e-01f,-1.7096189e-01f},
{-8.41555e-01f,5.4017144e-01f},{-2.5486565e-01f,9.6697646e-01f},{4.659765e-01f,8.847971e-01f},{1e+00f,-0e+00f},{7.347389e-01f,-6.7835003e-01f},
{7.968244e-02f,-9.968203e-01f},{-6.176473e-01f,-7.864552e-01f},{-9.873014e-01f,-1.5885815e-01f},{-8.331702e-01f,5.530167e-01f},
{-2.370236e-01f,9.715039e-01f},{4.8486924e-01f,8.7458664e-01f},{1e+00f,-0e+00f},{7.326543e-01f,-6.80601e-01f},{7.356457e-02f,-9.9729043e-01f},
{-6.248595e-01f,-7.807372e-01f},{-9.891765e-01f,-1.4673047e-01f},{-8.245893e-01f,5.657318e-01f},{-2.1910124e-01f,9.757021e-01f},
{5.0353837e-01f,8.6397284e-01f},{1e+00f,-0e+00f},{7.3056275e-01f,-6.8284553e-01f},{6.744392e-02f,-9.9772304e-01f},{-6.3201874e-01f,-7.749531e-01f},
{-9.9090266e-01f,-1.345807e-01f},{-8.1581444e-01f,5.7831377e-01f},{-2.0110464e-01f,9.795698e-01f},{5.219753e-01f,8.529606e-01f},{1e+00f,-0e+00f},
{7.2846437e-01f,-6.850837e-01f},{6.1320737e-02f,-9.981181e-01f},{-6.3912445e-01f,-7.6910335e-01f},{-9.9247956e-01f,-1.2241068e-01f},
{-8.068476e-01f,5.907597e-01f},{-1.8303989e-01f,9.831055e-01f},{5.4017144e-01f,8.41555e-01f},{1e+00f,-0e+00f},{7.263591e-01f,-6.8731534e-01f},
{5.5195246e-02f,-9.9847555e-01f},{-6.4617604e-01f,-7.631884e-01f},{-9.93907e-01f,-1.10222206e-01f},{-7.9769087e-01f,6.030666e-01f},
{-1.6491312e-01f,9.863081e-01f},{5.581185e-01f,8.297612e-01f},{1e+00f,-0e+00f},{7.242471e-01f,-6.8954057e-01f},{4.9067676e-02f,-9.9879545e-01f},
{-6.5317285e-01f,-7.572088e-01f},{-9.951847e-01f,-9.801714e-02f},{-7.883464e-01f,6.152316e-01f},{-1.4673047e-01f,9.891765e-01f},
{5.7580817e-01f,8.175848e-01f},{1e+00f,-0e+00f},{7.221282e-01f,-6.917592e-01f},{4.293826e-02f,-9.9907774e-01f},{-6.6011435e-01f,-7.5116515e-01f},
{-9.963126e-01f,-8.579731e-02f},{-7.788165e-01f,6.272518e-01f},{-1.284981e-01f,9.9170977e-01f},{5.932323e-01f,8.0503136e-01f},{1e+00f,-0e+00f},
{7.2000253e-01f,-6.9397146e-01f},{3.6807224e-02f,-9.9932235e-01f},{-6.6699994e-01f,-7.4505776e-01f},{-9.9729043e-01f,-7.356457e-02f},
{-7.6910335e-01f,6.3912445e-01f},{-1.10222206e-01f,9.93907e-01f},{6.103828e-01f,7.9210657e-01f},{1e+00f,-0e+00f},{7.1787006e-01f,-6.961771e-01f},
{3.0674804e-02f,-9.995294e-01f},{-6.73829e-01f,-7.388873e-01f},{-9.981181e-01f,-6.1320737e-02f},{-7.592092e-01f,6.5084666e-01f},
{-9.1908954e-02f,9.957674e-01f},{6.272518e-01f,7.788165e-01f},{1e+00f,-0e+00f},{7.1573085e-01f,-6.9837624e-01f},{2.4541229e-02f,-9.996988e-01f},
{-6.80601e-01f,-7.326543e-01f},{-9.9879545e-01f,-4.9067676e-02f},{-7.491364e-01f,6.624158e-01f},{-7.356457e-02f,9.9729043e-01f},
{6.4383155e-01f,7.6516724e-01f},{1e+00f,-0e+00f},{7.1358484e-01f,-7.005688e-01f},{1.840673e-02f,-9.998306e-01f},{-6.8731534e-01f,-7.263591e-01f},
{-9.9932235e-01f,-3.6807224e-02f},{-7.388873e-01f,6.73829e-01f},{-5.5195246e-02f,9.9847555e-01f},{6.6011435e-01f,7.5116515e-01f},{1e+00f,-0e+00f},
{7.114322e-01f,-7.0275474e-01f},{1.2271538e-02f,-9.999247e-01f},{-6.9397146e-01f,-7.2000253e-01f},{-9.996988e-01f,-2.4541229e-02f},
{-7.2846437e-01f,6.850837e-01f},{-3.6807224e-02f,9.9932235e-01f},{6.760927e-01f,7.368166e-01f},{1e+00f,-0e+00f},{7.092728e-01f,-7.0493406e-01f},
{6.1358847e-03f,-9.9998116e-01f},{-7.005688e-01f,-7.1358484e-01f},{-9.999247e-01f,-1.2271538e-02f},{-7.1787006e-01f,6.961771e-01f},
{-1.840673e-02f,9.998306e-01f},{6.917592e-01f,7.221282e-01f},
};

constexpr int S_ = 2048, D_ = 512, F_ = 1792, DF_ = 257;

constexpr size_t OFF_W1T = 0;
constexpr size_t OFF_W2T = OFF_W1T + (size_t)D_ * F_ * 2;
constexpr size_t OFF_WF  = OFF_W2T + (size_t)D_ * F_ * 2;
constexpr size_t OFF_G   = OFF_WF + (size_t)256 * 2048 * 4;
constexpr size_t OFF_T   = OFF_G + (size_t)2048 * 16;
constexpr size_t OFF_SC  = OFF_T + (size_t)258 * 8192;
constexpr size_t OFF_X1  = OFF_SC + 8192;
constexpr size_t MB_ = (size_t)1 << 20;
constexpr size_t OFF_XN  = OFF_X1 + 2 * MB_;
constexpr size_t OFF_H   = OFF_XN + 2 * MB_;
constexpr size_t OFF_X2  = OFF_H + 7 * MB_;
constexpr size_t OFF_CNT = OFF_X2 + 2 * MB_;
constexpr size_t WS_END  = OFF_CNT + 64 * 128;
constexpr int SPIN = 1 << 16;

#define STAMP_DECL
#define STAMP(i)
#define STAMP_END(kid)
#define KID_ARG
#define KID(x)

__device__ __forceinline__ float u2f(unsigned h) { return __builtin_bit_cast(float, h); }
__device__ __forceinline__ unsigned f2u(float h) { return __builtin_bit_cast(unsigned, h); }
__device__ __forceinline__ float bf2f(unsigned h) { return u2f(h << 16); }
__device__ __forceinline__ float bflo(unsigned p) { return u2f(p << 16); }
__device__ __forceinline__ float bfhi(unsigned p) { return u2f(p & 0xffff0000u); }
__device__ __forceinline__ unsigned pack_bf16(float lo, float hi) {
    const bfp2 r = __builtin_convertvector(f2{lo, hi}, bfp2);
    return __builtin_bit_cast(unsigned, r);
}
__device__ __forceinline__ float rbf(float x) { return bflo(pack_bf16(x, x)); }

#define SB() __builtin_amdgcn_sched_barrier(0)
#define BAR() do { asm volatile("s_waitcnt lgkmcnt(0)" ::: "memory"); __builtin_amdgcn_s_barrier(); asm volatile("" ::: "memory"); } while (0)
#define VMW(n) asm volatile("s_waitcnt vmcnt(%0)" :: "i"(n) : "memory")
#define TIE(x) asm volatile("" : "+v"(x))
#define TIEA(x) asm volatile("" : "+a"(x))
#define GLD1(dst, voff, sb, imm)   asm volatile("global_load_dword %0, %1, %2 offset:%3" : "=v"(dst) : "v"(voff), "s"(sb), "i"(imm) : "memory")
#define GLD1C(dst, voff, sb, imm)  asm volatile("global_load_dword %0, %1, %2 offset:%3 sc1" : "=v"(dst) : "v"(voff), "s"(sb), "i"(imm) : "memory")
#define GLD2(dst, voff, sb, imm)   asm volatile("global_load_dwordx2 %0, %1, %2 offset:%3" : "=v"(dst) : "v"(voff), "s"(sb), "i"(imm) : "memory")
#define GLD4(dst, voff, sb, imm)   asm volatile("global_load_dwordx4 %0, %1, %2 offset:%3" : "=v"(dst) : "v"(voff), "s"(sb), "i"(imm) : "memory")
#define GLD4C(dst, voff, sb, imm)  asm volatile("global_load_dwordx4 %0, %1, %2 offset:%3 sc1" : "=v"(dst) : "v"(voff), "s"(sb), "i"(imm) : "memory")
#define GLD4A(dst, voff, sb, imm)  asm volatile("global_load_dwordx4 %0, %1, %2 offset:%3" : "=a"(dst) : "v"(voff), "s"(sb), "i"(imm) : "memory")
#define GST4C(src, voff, sb, imm)  asm volatile("global_store_dwordx4 %1, %0, %2 offset:%3 sc1\n\ts_nop 1" :: "v"(src), "v"(voff), "s"(sb), "i"(imm) : "memory")
#define GST2C(src, voff, sb, imm)  asm volatile("global_store_dwordx2 %1, %0, %2 offset:%3 sc1" :: "v"(src), "v"(voff), "s"(sb), "i"(imm) : "memory")
#define GLDS(voff, sb, ldsb)       do { unsigned sv_; asm volatile("s_mov_b32 %0, m0\n\ts_mov_b32 m0, %3\n\ts_nop 0\n\tglobal_load_lds_dwordx4 %1, %2\n\ts_mov_b32 m0, %0" : "=&s"(sv_) : "v"(voff), "s"(sb), "s"(ldsb) : "memory"); } while (0)

#define OPTX2 0
#define OPTAUX 16
__device__ __forceinline__ unsigned nomark(unsigned v) { return v < 0xFFFFFFFEu ? v : 0xFFFFFFFEu; }
__device__ __forceinline__ unsigned umax3(unsigned a, unsigned b, unsigned c) { const unsigned m = a > b ? a : b; return m > c ? m : c; }
__device__ __forceinline__ unsigned umax5(unsigned m, u4 v) { return umax3(umax3(m, v[0], v[1]), v[2], v[3]); }
__device__ __forceinline__ bool any_mark(unsigned m) { return __builtin_amdgcn_ballot_w64(m == ~0u) != 0; }
__device__ __forceinline__ bool any_nan(float a) { return __builtin_amdgcn_ballot_w64(a != a) != 0; }
__device__ __forceinline__ void st_wt(void* base, unsigned off, u4 v) {
    const __amdgpu_buffer_rsrc_t rs = __builtin_amdgcn_make_buffer_rsrc(base, 0, 0x7fffffff, 0x00020000);
    __builtin_amdgcn_raw_buffer_store_b128(v, rs, off, 0, 16);
}
#define OPTCODE 3
#define SYM_COL "_Z5k_colPhPKtS1_"
#define SYM_ROW "_Z8k_rowfwdPKtPhS0_S0_S0_S0_S0_S0_S0_"
#define SYM_FUSED "_Z7k_fusedPKtPhS0_S0_Pti"
#define CODE_ADDR(p, SYM) do { unsigned lo_, hi_; asm volatile("s_getpc_b64 s[40:41]\n\ts_add_u32 s40, s40, " SYM "@rel32@lo+4\n\ts_addc_u32 s41, s41, " SYM "@rel32@hi+12\n\t" \
        "s_mov_b32 %0, s40\n\ts_mov_b32 %1, s41" : "=s"(lo_), "=s"(hi_) :: "s40", "s41", "scc"); p = (const unsigned*)(((unsigned long long)hi_ << 32) | lo_); } while (0)
__device__ __forceinline__ f2 ld_tw(const float2c* p) { return *reinterpret_cast<const f2*>(p); }
__device__ __forceinline__ f2 cmul(f2 a, f2 w) {
    f2 r;
    asm("v_pk_mul_f32 %0, %1, %2 op_sel:[0,0] op_sel_hi:[1,0]" : "=v"(r) : "v"(a), "v"(w));
    asm("v_pk_fma_f32 %0, %1, %2, %0 op_sel:[1,1,0] op_sel_hi:[0,1,1] neg_lo:[0,1,0]" : "+v"(r) : "v"(a), "v"(w));
    return r;
}
__device__ __forceinline__ f2 cmulc(f2 a, f2 w) {
    f2 r;
    asm("v_pk_mul_f32 %0, %1, %2 op_sel:[0,0] op_sel_hi:[1,0]" : "=v"(r) : "v"(a), "v"(w));
    asm("v_pk_fma_f32 %0, %1, %2, %0 op_sel:[1,1,0] op_sel_hi:[0,1,1] neg_hi:[0,1,0]" : "+v"(r) : "v"(a), "v"(w));
    return r;
}
__device__ __forceinline__ f2 cmul_s(f2 a, f2 w) {
    f2 r;
    asm("v_pk_mul_f32 %0, %1, %2 op_sel:[0,0] op_sel_hi:[1,0]" : "=v"(r) : "v"(a), "s"(w));
    asm("v_pk_fma_f32 %0, %1, %2, %0 op_sel:[1,1,0] op_sel_hi:[0,1,1] neg_lo:[0,1,0]" : "+v"(r) : "v"(a), "s"(w));
    return r;
}
__device__ __forceinline__ f2 cmulc_s(f2 a, f2 w) {
    f2 r;
    asm("v_pk_mul_f32 %0, %1, %2 op_sel:[0,0] op_sel_hi:[1,0]" : "=v"(r) : "v"(a), "s"(w));
    asm("v_pk_fma_f32 %0, %1, %2, %0 op_sel:[1,1,0] op_sel_hi:[0,1,1] neg_hi:[0,1,0]" : "+v"(r) : "v"(a), "s"(w));
    return r;
}
__device__ __forceinline__ f2 cm_a(f2 a, f2 w) { f2 r; asm("v_pk_mul_f32 %0, %1, %2 op_sel:[0,0] op_sel_hi:[1,0]" : "=v"(r) : "v"(a), "v"(w)); return r; }
__device__ __forceinline__ f2 cm_as(f2 a, f2 w) { f2 r; asm("v_pk_mul_f32 %0, %1, %2 op_sel:[0,0] op_sel_hi:[1,0]" : "=v"(r) : "v"(a), "s"(w)); return r; }
template <bool CJ> __device__ __forceinline__ f2 cm_b(f2 a, f2 w, f2 r) {
    if (CJ) asm("v_pk_fma_f32 %0, %1, %2, %0 op_sel:[1,1,0] op_sel_hi:[0,1,1] neg_hi:[0,1,0]" : "+v"(r) : "v"(a), "v"(w));
    else    asm("v_pk_fma_f32 %0, %1, %2, %0 op_sel:[1,1,0] op_sel_hi:[0,1,1] neg_lo:[0,1,0]" : "+v"(r) : "v"(a), "v"(w));
    return r;
}
template <bool CJ> __device__ __forceinline__ f2 cm_bs(f2 a, f2 w, f2 r) {
    if (CJ) asm("v_pk_fma_f32 %0, %1, %2, %0 op_sel:[1,1,0] op_sel_hi:[0,1,1] neg_hi:[0,1,0]" : "+v"(r) : "v"(a), "s"(w));
    else    asm("v_pk_fma_f32 %0, %1, %2, %0 op_sel:[1,1,0] op_sel_hi:[0,1,1] neg_lo:[0,1,0]" : "+v"(r) : "v"(a), "s"(w));
    return r;
}
template <bool CJ, int C0> __device__ __forceinline__ void cmul8(f2 (&a)[8], const f2 (&w)[8]) {
    f2 r[8];
#pragma unroll
    for (int c = C0; c < 8; ++c) r[c] = cm_a(a[c], w[c]);
#pragma unroll
    for (int c = C0; c < 8; ++c) a[c] = cm_b<CJ>(a[c], w[c], r[c]);
}
template <bool CJ> __device__ __forceinline__ void cmul8s(f2 (&a)[8], const f2 (&w)[8]) {
    f2 r[8];
#pragma unroll
    for (int c = 1; c < 8; ++c) r[c] = cm_as(a[c], w[c]);
#pragma unroll
    for (int c = 1; c < 8; ++c) a[c] = cm_bs<CJ>(a[c], w[c], r[c]);
}
__device__ __forceinline__ void powers8(f2 b, f2 (&w)[8]) {
    w[0] = f2{1.f, 0.f}; w[1] = b;
    w[2] = cmul(b, b); w[3] = cmul(w[2], b); w[4] = cmul(w[2], w[2]);
    w[5] = cmul(w[4], b); w[6] = cmul(w[3], w[3]); w[7] = cmul(w[4], w[3]);
}
__device__ __forceinline__ unsigned pack_h(f2 a, float s) {
    unsigned d; asm("v_fma_mixlo_f16 %0, %1, %3, 0\n\tv_fma_mixhi_f16 %0, %2, %3, 0" : "=&v"(d) : "v"(a.x), "v"(a.y), "v"(s)); return d;
}
__device__ __forceinline__ float mixl(unsigned h, float s, float c) { float r; asm("v_fma_mix_f32 %0, %1, %2, %3 op_sel_hi:[1,0,0]" : "=v"(r) : "v"(h), "v"(s), "v"(c)); return r; }
__device__ __forceinline__ float mixh(unsigned h, float s, float c) { float r; asm("v_fma_mix_f32 %0, %1, %2, %3 op_sel:[1,0,0] op_sel_hi:[1,0,0]" : "=v"(r) : "v"(h), "v"(s), "v"(c)); return r; }
__device__ __forceinline__ float mixl0(unsigned h, float s) { float r; asm("v_fma_mix_f32 %0, %1, %2, 0 op_sel_hi:[1,0,0]" : "=v"(r) : "v"(h), "v"(s)); return r; }
__device__ __forceinline__ float mixh0(unsigned h, float s) { float r; asm("v_fma_mix_f32 %0, %1, %2, 0 op_sel:[1,0,0] op_sel_hi:[1,0,0]" : "=v"(r) : "v"(h), "v"(s)); return r; }
__device__ __forceinline__ void pair_swap(const u4& a, const u4& b, u4& x, u4& y, bool odd) {
#pragma unroll
    for (int i = 0; i < 4; ++i) {
        const unsigned pb = __builtin_amdgcn_mov_dpp(b[i], 0xB1, 0xf, 0xf, true), pa = __builtin_amdgcn_mov_dpp(a[i], 0xB1, 0xf, 0xf, true);
        x[i] = odd ? pb : a[i];
        y[i] = odd ? b[i] : pa;
    }
}
__device__ __forceinline__ f2 cis(float x) { return f2{__builtin_amdgcn_cosf(x), __builtin_amdgcn_sinf(-x)}; }
__device__ __forceinline__ void tw8(float x, f2 (&w)[8]) {
    w[0] = f2{1.f, 0.f};
#pragma unroll
    for (int i = 1; i < 8; ++i) w[i] = cis(x * (float)i);
}
__device__ __forceinline__ f2 add_mi(f2 a, f2 b) {
    f2 r; asm("v_pk_add_f32 %0, %1, %2 op_sel:[0,1] op_sel_hi:[1,0] neg_hi:[0,1]" : "=v"(r) : "v"(a), "v"(b)); return r;
}
__device__ __forceinline__ f2 add_pi(f2 a, f2 b) {
    f2 r; asm("v_pk_add_f32 %0, %1, %2 op_sel:[0,1] op_sel_hi:[1,0] neg_lo:[0,1]" : "=v"(r) : "v"(a), "v"(b)); return r;
}
__device__ __forceinline__ f2 add_cj(f2 a, f2 b) {
    f2 r; asm("v_pk_add_f32 %0, %1, %2 neg_hi:[0,1]" : "=v"(r) : "v"(a), "v"(b)); return r;
}
__device__ __forceinline__ f2 sub_cj(f2 a, f2 b) {
    f2 r; asm("v_pk_add_f32 %0, %1, %2 neg_lo:[0,1]" : "=v"(r) : "v"(a), "v"(b)); return r;
}
template <bool INV> __device__ __forceinline__ f2 addr(f2 a, f2 b) { return INV ? add_pi(a, b) : add_mi(a, b); }
template <bool INV> __device__ __forceinline__ f2 subr(f2 a, f2 b) { return INV ? add_mi(a, b) : add_pi(a, b); }

template <bool INV> __device__ __forceinline__ void fft4(f2& a0, f2& a1, f2& a2, f2& a3) {
    f2 t0 = a0 + a2, t1 = a0 - a2, t2 = a1 + a3, d = a1 - a3;
    a0 = t0 + t2; a2 = t0 - t2; a1 = addr<INV>(t1, d); a3 = subr<INV>(t1, d);
}
template <bool INV> __device__ __forceinline__ void fft8(f2 (&a)[8]) {
    const float R = 0.70710678118654752440f;
    f2 e0 = a[0], e1 = a[2], e2 = a[4], e3 = a[6];
    f2 o0 = a[1], o1 = a[3], o2 = a[5], o3 = a[7];
    fft4<INV>(e0, e1, e2, e3);
    fft4<INV>(o0, o1, o2, o3);
    const f2 q1 = addr<INV>(o1, o1), q3 = subr<INV>(o3, o3);
    const f2 RR = {R, R}, NR = {-R, -R};
    a[0] = e0 + o0; a[4] = e0 - o0;
    a[1] = __builtin_elementwise_fma(q1, RR, e1); a[5] = __builtin_elementwise_fma(q1, NR, e1);
    a[2] = addr<INV>(e2, o2); a[6] = subr<INV>(e2, o2);
    a[3] = __builtin_elementwise_fma(q3, NR, e3); a[7] = __builtin_elementwise_fma(q3, RR, e3);
}
#define WAVE_SYNC() do { __builtin_amdgcn_fence(__ATOMIC_RELEASE, "wavefront"); __builtin_amdgcn_wave_barrier(); __builtin_amdgcn_fence(__ATOMIC_ACQUIRE, "wavefront"); } while (0)

constexpr int XR = 320;

__device__ __forceinline__ void fft256_fwd(f2 (&a)[8], f2* L, int t, const f2 (&w1)[8], const f2 (&w2)[8]) {
    fft8<false>(a);
    cmul8<false, 1>(a, w1);
#pragma unroll
    for (int c = 0; c < 8; ++c) L[c * 36 + t] = a[c];
    WAVE_SYNC();
    const int c1 = t >> 2, a4 = t & 3;
#pragma unroll
    for (int m = 0; m < 8; ++m) a[m] = L[c1 * 36 + a4 + 4 * m];
    WAVE_SYNC();
    fft8<false>(a);
    cmul8<false, 1>(a, w2);
#pragma unroll
    for (int c = 0; c < 8; ++c) L[c1 * 34 + c * 4 + a4] = a[c];
    WAVE_SYNC();
#pragma unroll
    for (int i = 0; i < 4; ++i) {
        f4 v = *reinterpret_cast<const f4*>(&L[c1 * 34 + a4 * 8 + 2 * i]);
        a[2 * i] = v.xy; a[2 * i + 1] = v.zw;
    }
    WAVE_SYNC();
    fft4<false>(a[0], a[1], a[2], a[3]);
    fft4<false>(a[4], a[5], a[6], a[7]);
}
__device__ __forceinline__ void fft256_inv(f2 (&a)[8], f2* L, int t, const f2 (&w1)[8], const f2 (&w2)[8]) {
    const int c1 = t >> 2, a4 = t & 3;
    fft4<true>(a[0], a[1], a[2], a[3]);
    fft4<true>(a[4], a[5], a[6], a[7]);
#pragma unroll
    for (int i = 0; i < 4; ++i) {
        f4 v; v.xy = a[2 * i]; v.zw = a[2 * i + 1];
        *reinterpret_cast<f4*>(&L[c1 * 34 + a4 * 8 + 2 * i]) = v;
    }
    WAVE_SYNC();
#pragma unroll
    for (int c = 0; c < 8; ++c) a[c] = L[c1 * 34 + c * 4 + a4];
    WAVE_SYNC();
    cmul8<true, 1>(a, w2);
    fft8<true>(a);
#pragma unroll
    for (int m = 0; m < 8; ++m) L[c1 * 36 + a4 + 4 * m] = a[m];
    WAVE_SYNC();
#pragma unroll
    for (int c = 0; c < 8; ++c) a[c] = L[c * 36 + t];
    WAVE_SYNC();
    cmul8<true, 1>(a, w1);
    fft8<true>(a);
}
__device__ __forceinline__ int zphys(int k) { return k + 4 * (k >> 4); }

__device__ __forceinline__ float half_sum(float v) {
    v += u2f(__builtin_amdgcn_update_dpp(0, (int)f2u(v), 0xB1, 0xf, 0xf, true));
    v += u2f(__builtin_amdgcn_update_dpp(0, (int)f2u(v), 0x4E, 0xf, 0xf, true));
    v += u2f(__builtin_amdgcn_update_dpp(0, (int)f2u(v), 0x141, 0xf, 0xf, true));
    v += u2f(__builtin_amdgcn_update_dpp(0, (int)f2u(v), 0x140, 0xf, 0xf, true));
    const unsigned x = f2u(v);
    const u2 r = __builtin_amdgcn_permlane16_swap(x, x, false, false);
    const unsigned r0 = r.x, r1 = r.y;
    return u2f(r0) + u2f(r1);
}
__device__ __forceinline__ void layernorm16(const f2 (&a)[8], unsigned (&o)[8]) {
    f2 s2 = (a[0] + a[1]) + (a[2] + a[3]) + ((a[4] + a[5]) + (a[6] + a[7]));
    const float mean = half_sum(s2.x + s2.y) * (1.0f / 512.0f);
    f2 q2 = {0.f, 0.f};
#pragma unroll
    for (int m = 0; m < 8; ++m) { f2 d = a[m] - mean; q2 = __builtin_elementwise_fma(d, d, q2); }
    const float q = half_sum(q2.x + q2.y);
    const float mb = rbf(mean);
    const float vb = rbf(q * (1.0f / 512.0f));
    const float den = rbf(__builtin_sqrtf(rbf(vb + 1e-5f)));
    const float rinv = 1.0f / den;
#pragma unroll
    for (int m = 0; m < 8; ++m) {
        const f2 d = a[m] - mb;
        const unsigned dp = pack_bf16(d.x, d.y);
        const f2 r = f2{bflo(dp), bfhi(dp)} * rinv;
        o[m] = pack_bf16(r.x, r.y);
    }
}

__device__ __forceinline__ void rowfwd_body(const unsigned (&raw)[8], const f2 (&w1t)[8], const f2 (&w2t)[8], f2 w512,
                                            const f2 (&tws)[8], f2* L0, u8* stg, int s0, u8* __restrict__ ws) {
    const int tid = threadIdx.x, j = tid >> 5, t = tid & 31, lane = tid & 63;
    f2 a[8];
#pragma unroll
    for (int m = 0; m < 8; ++m) a[m] = f2{bflo(raw[m]), bfhi(raw[m])};
    unsigned xn[8];
    layernorm16(a, xn);
#pragma unroll
    for (int m = 0; m < 8; ++m) a[m] = f2{bflo(xn[m]), bfhi(xn[m])};
    f2* L = L0 + j * XR;
    fft256_fwd(a, L, t, w1t, w2t);
    {
        f2* Z = L + 20 * (t & 3) + (t >> 2);
#pragma unroll
        for (int i = 0; i < 8; ++i) Z[80 * (i & 3) + 8 * (i >> 2)] = a[i];
    }
    BAR();
    const int k = tid, km = (256 - k) & 255;
    {
        const f2* Zk = L0 + zphys(k);
        const f2* Zm = L0 + zphys(km);
        f2 sm[8], df[8], r[8];
#pragma unroll
        for (int jj = 0; jj < 8; ++jj) {
            const f2 zk = Zk[jj * XR], zm = Zm[jj * XR];
            sm[jj] = add_cj(zk, zm); df[jj] = sub_cj(zk, zm);
        }
#pragma unroll
        for (int jj = 0; jj < 8; ++jj) r[jj] = cm_a(df[jj], w512);
#pragma unroll
        for (int jj = 0; jj < 8; ++jj) r[jj] = cm_b<false>(df[jj], w512, r[jj]);
#pragma unroll
        for (int jj = 0; jj < 8; ++jj) {
            a[jj] = add_mi(sm[jj], r[jj]);
            if (k == 0) a[jj].y = sm[jj].x - df[jj].y;
        }
    }
    fft8<false>(a);
    cmul8s<false>(a, tws);
    u4 lo, hi, s0v, s1v;
#pragma unroll
    for (int q = 0; q < 4; ++q) { lo[q] = pack_h(a[q], 4.f); hi[q] = pack_h(a[4 + q], 4.f); }
    pair_swap(lo, hi, s0v, s1v, lane & 1);
    u8* dst = ws + OFF_T + (size_t)(tid & ~1) * 8192 + s0 * 32 + (lane & 1) * 16;
    *reinterpret_cast<u4*>(dst) = s0v;
    *reinterpret_cast<u4*>(dst + 8192) = s1v;
}
__device__ __forceinline__ void split_tw(const f4 (&q)[4], f2 (&w)[8]) {
#pragma unroll
    for (int i = 0; i < 4; ++i) { w[2 * i] = q[i].xy; w[2 * i + 1] = q[i].zw; }
}

constexpr int NPREP_WF = 256, NPREP_G = 32, NPREP_W1 = OPTW, NPREP_W2 = 512 - OPTW;
__device__ __forceinline__ void prep_w1(int pb, u16* s, u8* __restrict__ ws, const u16* __restrict__ w1) {
    const int tid = threadIdx.x, c = pb >> 4, ks = pb & 15;
    u4 v[2];
#pragma unroll
    for (int r = 0; r < 2; ++r) {
        const int i = min(tid + 256 * r, 447), kk = i / 14, ch = i - kk * 14;
        v[r] = *reinterpret_cast<const u4*>(w1 + (size_t)(32 * ks + kk) * F_ + 112 * c + ch * 8);
    }
#pragma unroll
    for (int r = 0; r < 2; ++r) {
        const int i = min(tid + 256 * r, 447), kk = i / 14, ch = i - kk * 14;
        *reinterpret_cast<u4*>(s + kk * 120 + ch * 8) = v[r];
    }
    __syncthreads();
#pragma unroll
    for (int r = 0; r < 2; ++r) {
        const int i = tid + 256 * r;
        if (i < 448) {
            const int n = i >> 6, lane = i & 63, kq = lane >> 4, ii = lane & 15;
            const int fl = 28 * (ii >> 2) + 4 * n + (ii & 3), k0 = 8 * kq;
            u4 o;
#pragma unroll
            for (int jj = 0; jj < 4; ++jj)
                o[jj] = (unsigned)s[(k0 + 2 * jj) * 120 + fl] | ((unsigned)s[(k0 + 2 * jj + 1) * 120 + fl] << 16);
            st_wt(ws + OFF_W1T, ((c * 16 + ks) * 7 * 64 + i) * 16, o);
        }
    }
}
__device__ __forceinline__ void prep_w2(int pb, u16* s, u8* __restrict__ ws, const u16* __restrict__ w2) {
    const int tid = threadIdx.x, c = pb >> 4, db = pb & 15;
    u4 v[2];
#pragma unroll
    for (int r = 0; r < 2; ++r) {
        const int i = min(tid + 256 * r, 447), ff = i >> 2, ch = i & 3;
        v[r] = *reinterpret_cast<const u4*>(w2 + (size_t)(112 * c + ff) * D_ + 32 * db + ch * 8);
    }
#pragma unroll
    for (int r = 0; r < 2; ++r) {
        const int i = min(tid + 256 * r, 447), ff = i >> 2, ch = i & 3;
        *reinterpret_cast<u4*>(s + ff * 40 + ch * 8) = v[r];
    }
    __syncthreads();
#pragma unroll
    for (int r = 0; r < 2; ++r) {
        const int i = tid + 256 * r;
        if (i < 448) {
            const int sidx = i >> 6, lane = i & 63, hh = lane >> 5, rr = lane & 31;
            const int cid = 2 * sidx + hh, g = cid & 3, ch = cid >> 2;
            int fl[8];
#pragma unroll
            for (int jj = 0; jj < 8; ++jj)
                fl[jj] = cid < 12 ? 28 * g + 8 * ch + jj : 28 * ((cid - 12) * 2 + (jj >> 2)) + 24 + (jj & 3);
            u4 o;
#pragma unroll
            for (int jj = 0; jj < 4; ++jj)
                o[jj] = (unsigned)s[fl[2 * jj] * 40 + rr] | ((unsigned)s[fl[2 * jj + 1] * 40 + rr] << 16);
            st_wt(ws + OFF_W2T, ((db * 112 + c * 7 + sidx) * 64 + lane) * 16, o);
        }
    }
}
__global__ __launch_bounds__(256) void k_rowfwd(const u16* __restrict__ xin, u8* __restrict__ ws,
                                                 const u16* __restrict__ wr, const u16* __restrict__ wi, const u16* __restrict__ w1,
                                                 const u16* w2, const u16* b1, const u16* b2, const u16* out KID_ARG) {
    __shared__ __attribute__((aligned(16))) u8 smem[8 * XR * 8 + 16384];
    STAMP_DECL STAMP(0);
    const int tid = threadIdx.x;
    if (blockIdx.x >= 256) {
        int pb = blockIdx.x - 256;
        if (pb < NPREP_WF) {
            const int tp = pb >> 4, b = (pb >> 3) & 1, p = ((pb >> 2) & 1) * 4 + (tid >> 6);
            const int k = (pb & 3) * 64 + (tid & 63);
            const int src = (256 * (tp & 1) + 64 * b + 8 * (tp >> 1) + p) * DF_ + k;
            u4 v[2];
#pragma unroll
            for (int x = 0; x < 2; ++x)
#pragma unroll
                for (int c3 = 0; c3 < 4; ++c3)
                    v[x][c3] = (unsigned)wr[src + (512 * c3 + 128 * x) * DF_] | ((unsigned)wi[src + (512 * c3 + 128 * x) * DF_] << 16);
            u4* WF = reinterpret_cast<u4*>(ws + OFF_WF) + (size_t)k * 512 + b * 256 + p * 32 + 2 * tp;
            WF[0] = v[0]; WF[1] = v[1];
        } else if (pb < NPREP_WF + NPREP_G) {
            pb -= NPREP_WF;
            if (tid >= 64) return;
            const int pos = pb * 64 + tid;
            const int idx = pos >> 8, p = (pos >> 5) & 7, tt = pos & 31;
            const int c1 = tt >> 2, e = tt & 3, b = idx >> 2, c3 = idx & 3;
            const int u = 8 * (64 * c3 + 8 * (2 * e + b) + c1) + p, mu = (2048 - u) & 2047;
            float ar = bf2f(wr[u * DF_]), ai = bf2f(wi[u * DF_]);
            float amr = bf2f(wr[mu * DF_]), ami = bf2f(wi[mu * DF_]);
            float br = bf2f(wr[u * DF_ + 256]), bi = bf2f(wi[u * DF_ + 256]);
            float bmr = bf2f(wr[mu * DF_ + 256]), bmi = bf2f(wi[mu * DF_ + 256]);
            float asr = 0.5f * (ar + amr), asi = 0.5f * (ai - ami);
            float bsr = 0.5f * (br + bmr), bsi = 0.5f * (bi - bmi);
            f2* G = reinterpret_cast<f2*>(ws + OFF_G) + ((idx >> 1) * 256 + (pos & 255)) * 2 + (idx & 1);
            G[0] = f2{0.5f * (asr + bsr), 0.5f * (asi + bsi)};
            G[2048] = f2{0.5f * (asr - bsr), 0.5f * (asi - bsi)};
            if (pb == 0 && tid < 64) *reinterpret_cast<unsigned*>(ws + OFF_CNT + tid * 128) = 0u;
        } else {
            prep_w1(pb - NPREP_WF - NPREP_G, reinterpret_cast<u16*>(smem), ws, w1);
        }
        STAMP_END(kid);
        return;
    }
    const int j = tid >> 5, t = tid & 31;
    const int s0 = (blockIdx.x & 7) * 32 + (blockIdx.x >> 3);
    unsigned raw[8], cw;
    {
        const unsigned xo = (unsigned)(s0 + 256 * j) * 1024u + t * 4;
#pragma unroll
        for (int m = 0; m < 8; ++m) GLD1(raw[m], xo, xin, 128 * m);
        const unsigned idx = blockIdx.x >> 3;
        const unsigned* cp; CODE_ADDR(cp, SYM_COL);
        const u8* ca = reinterpret_cast<const u8*>(cp) + (idx * 2 + (tid & 1)) * 128;
        if (tid >= 2 && tid < 13) ca = ws + min((idx * 11 + tid - 2) * 65536u, (unsigned)WS_END - 4u);
        const unsigned* cr; CODE_ADDR(cr, SYM_ROW);
        if (tid >= 16 && tid < 18) ca = reinterpret_cast<const u8*>(cr) + (idx * 2 + (tid & 1)) * 128;
        if (tid == 13) ca = reinterpret_cast<const u8*>(out) + idx * 65536u;
        if (tid == 14) ca = reinterpret_cast<const u8*>(w2) + min(idx * 65536u, (unsigned)(D_ * F_ * 2 - 4));
        if (tid == 15) ca = reinterpret_cast<const u8*>((idx & 1) ? b2 : b1);
        asm volatile("global_load_dword %0, %1, off" : "=v"(cw) : "v"(ca) : "memory");
    }
    f4 ts4[4];
#pragma unroll
    for (int i = 0; i < 4; ++i) ts4[i] = reinterpret_cast<const f4*>(TWS)[s0 * 4 + i];
    f2 w1t[8], w2t[8], tws[8];
    split_tw(ts4, tws);
    tw8(t * (1.f / 256), w1t); tw8((t & 3) * (1.f / 32), w2t);
    f2 w512 = cis(tid * (1.f / 512));
#pragma unroll
    for (int i = 0; i < 8; ++i) { TIE(w1t[i]); TIE(w2t[i]); }
    TIE(w512);
    VMW(1);
#pragma unroll
    for (int m = 0; m < 8; ++m) TIE(raw[m]);
    rowfwd_body(raw, w1t, w2t, w512, tws, reinterpret_cast<f2*>(smem), smem + 8 * XR * 8, s0, ws);
    VMW(2); TIE(cw);
    STAMP_END(kid);
}

__global__ __launch_bounds__(256) void k_col(u8* __restrict__ ws, const u16* __restrict__ w2, const u16* __restrict__ w1 KID_ARG) {
    __shared__ __attribute__((aligned(16))) f2 smem[8 * XR + 2048];
    STAMP_DECL STAMP(0);
    const int tid = threadIdx.x;
    if (blockIdx.x >= 256) {
        if (blockIdx.x < 512) prep_w2(blockIdx.x - 256, reinterpret_cast<u16*>(smem), ws, w2);
        else prep_w1(blockIdx.x - 512 + NPREP_W1, reinterpret_cast<u16*>(smem), ws, w1);
        STAMP_END(kid);
        return;
    }
    const int p = tid >> 5, t = tid & 31, pa = tid & 7, ta = tid >> 3, k = blockIdx.x;
    unsigned* T = reinterpret_cast<unsigned*>(ws + OFF_T) + (size_t)k * 2048 + tid;
    f2 a[8];
    unsigned ah[8];
    u4 q[8];
    unsigned cw, cw2;
    {
        const unsigned to = tid * 4;
#pragma unroll
        for (int m = 0; m < 8; ++m) GLD1(ah[m], to, ws + OFF_T + (size_t)k * 8192 + 4096, m * 1024 - 4096);
        const unsigned q0 = tid * 16;
        asm volatile("s_cmp_lg_u32 %10, 0\n\ts_cbranch_scc1 .Lgskip%=\n\t"
                     "global_load_dwordx4 %0, %6, %7 offset:-4096\n\tglobal_load_dwordx4 %1, %6, %7\n\tglobal_load_dwordx4 %2, %6, %8 offset:-4096\n\t"
                     "global_load_dwordx4 %3, %6, %8\n\tglobal_load_dwordx4 %4, %6, %9 offset:-4096\n\tglobal_load_dwordx4 %5, %6, %9\n.Lgskip%=:"
                     : "=&v"(q[2]), "=&v"(q[3]), "=&v"(q[4]), "=&v"(q[5]), "=&v"(q[6]), "=&v"(q[7])
                     : "v"(q0), "s"(ws + OFF_G + 3 * 4096), "s"(ws + OFF_G + 5 * 4096), "s"(ws + OFF_G + 7 * 4096), "s"(k) : "memory", "scc");
    }
    unsigned sc0;
    asm volatile("s_load_dword %0, %1, 0x0" : "=&s"(sc0) : "s"(ws + OFF_SC + k * 4) : "memory");
    f2 w1t[8], w2t[8];
    f2* const L = smem + p * (XR + 4);
    f2* const LA = smem + pa * (XR + 4) + ta;
    unsigned* const LE = reinterpret_cast<unsigned*>(smem + 8 * (XR + 4));
    const int c1 = t >> 2, a4 = t & 3;
    tw8(ta * (1.f / 256), w1t); tw8(a4 * (1.f / 32), w2t);
#pragma unroll
    for (int i = 0; i < 8; ++i) { TIE(w1t[i]); TIE(w2t[i]); }
    {
        const u8* qb = (k ? ws + OFF_WF + (size_t)k * 8192 : ws + OFF_G) + 4096;
        const unsigned q0 = tid * 16;
        GLD4(q[0], q0, qb, -4096);
        GLD4(q[1], q0, qb, 0);
        const unsigned* cp; CODE_ADDR(cp, SYM_FUSED);
        const unsigned* cc; CODE_ADDR(cc, SYM_COL);
        const unsigned co = ((k >> 3) * 5 + min(tid, 4)) * 128, c2 = ((tid >> 6) * 16 + (tid & 15)) * 128;
        GLD1(cw, co, cp, 0);
        asm volatile("s_cmp_lg_u32 %3, 0\n\ts_cbranch_scc1 .Lcskip%=\n\tglobal_load_dword %0, %1, %2\n.Lcskip%=:" : "=&v"(cw2) : "v"(c2), "s"(cc), "s"(k) : "memory", "scc");
    }
    if (k) VMW(3); else VMW(10);
#pragma unroll
    for (int m = 0; m < 8; ++m) TIE(ah[m]);
    {
        const u4 one = {~0u, ~0u, ~0u, ~0u};
        const unsigned mo = ((k & 7) * 32 + (k >> 3)) * 8192 + tid * 16;
        GST4C(one, mo, ws + OFF_XN + 4096, -4096);
        GST4C(one, mo, ws + OFF_XN + 4096, 0);
    }
#pragma unroll
    for (int m = 0; m < 8; ++m) a[m] = f2{mixl0(ah[m], 0.25f), mixh0(ah[m], 0.25f)};
    STAMP(1);
    fft8<false>(a);
    cmul8<false, 1>(a, w1t);
#pragma unroll
    for (int c = 0; c < 8; ++c) LA[c * 36] = a[c];
    BAR();
#pragma unroll
    for (int m = 0; m < 8; ++m) a[m] = L[c1 * 36 + a4 + 4 * m];
    WAVE_SYNC();
    fft8<false>(a);
    cmul8<false, 1>(a, w2t);
#pragma unroll
    for (int c = 0; c < 8; ++c) L[c1 * 34 + c * 4 + a4] = a[c];
    WAVE_SYNC();
#pragma unroll
    for (int i = 0; i < 4; ++i) {
        f4 v = *reinterpret_cast<const f4*>(&L[c1 * 34 + a4 * 8 + 2 * i]);
        a[2 * i] = v.xy; a[2 * i + 1] = v.zw;
    }
    WAVE_SYNC();
    fft4<false>(a[0], a[1], a[2], a[3]);
    fft4<false>(a[4], a[5], a[6], a[7]);
    SB();
    STAMP(2);
    f2 zc[8];
    {
        const int qb0 = 16 * a4 + c1;
        const unsigned lw = (unsigned)(size_t)(__attribute__((address_space(3))) f2*)(L + qb0);
        const unsigned lr = (unsigned)(size_t)(__attribute__((address_space(3))) f2*)(smem + ((8 - p) & 7) * (XR + 4) + (p ? 255 : 256) - 200 - qb0);
        const unsigned lx = t ? lw : lw + 2048;
        asm volatile("s_cmp_lg_u32 %19, 0\n\ts_cbranch_scc1 .Lxskip%=\n\t"
                     "ds_write_b64 %16, %8\n\tds_write_b64 %16, %9 offset:512\n\tds_write_b64 %16, %10 offset:1024\n\tds_write_b64 %16, %11 offset:1536\n\t"
                     "ds_write_b64 %16, %12 offset:64\n\tds_write_b64 %16, %13 offset:576\n\tds_write_b64 %16, %14 offset:1088\n\tds_write_b64 %16, %15 offset:1600\n\t"
                     "ds_write_b64 %18, %8\n\ts_waitcnt lgkmcnt(0)\n\ts_barrier\n\t"
                     "ds_read_b64 %0, %17 offset:1600\n\tds_read_b64 %1, %17 offset:1088\n\tds_read_b64 %2, %17 offset:576\n\tds_read_b64 %3, %17 offset:64\n\t"
                     "ds_read_b64 %4, %17 offset:1536\n\tds_read_b64 %5, %17 offset:1024\n\tds_read_b64 %6, %17 offset:512\n\tds_read_b64 %7, %17\n.Lxskip%=:"
                     : "=&v"(zc[0]), "=&v"(zc[1]), "=&v"(zc[2]), "=&v"(zc[3]), "=&v"(zc[4]), "=&v"(zc[5]), "=&v"(zc[6]), "=&v"(zc[7])
                     : "v"(a[0]), "v"(a[1]), "v"(a[2]), "v"(a[3]), "v"(a[4]), "v"(a[5]), "v"(a[6]), "v"(a[7]), "v"(lw), "v"(lr), "v"(lx), "s"(k) : "memory", "scc");
    }
    if (k) VMW(3); else VMW(4);
    STAMP(3);
#pragma unroll
    for (int i = 0; i < 8; ++i) TIE(q[i]);
    {
        f2 fw[8];
        if (k != 0) {
#pragma unroll
            for (int i = 0; i < 4; ++i) { fw[i] = f2{bflo(q[0][i]), bfhi(q[0][i])}; fw[4 + i] = f2{bflo(q[1][i]), bfhi(q[1][i])}; }
        } else {
#pragma unroll
            for (int i = 0; i < 4; ++i) { fw[2 * i] = f2{u2f(q[i][0]), u2f(q[i][1])}; fw[2 * i + 1] = f2{u2f(q[i][2]), u2f(q[i][3])}; }
        }
        cmul8<false, 0>(a, fw);
#define XF1(r, z, g) "v_pk_fma_f32 " r ", " z ", " g ", " r " op_sel:[0,0,0] op_sel_hi:[0,1,1]\n\t"
#define XF2(r, z, g) "v_pk_fma_f32 " r ", " z ", " g ", " r " op_sel:[1,1,0] op_sel_hi:[1,0,1] neg_hi:[0,1,0]\n\t"
        {
            f2 g2[8];
#pragma unroll
            for (int i = 0; i < 4; ++i) { g2[2 * i] = f2{u2f(q[4 + i][0]), u2f(q[4 + i][1])}; g2[2 * i + 1] = f2{u2f(q[4 + i][2]), u2f(q[4 + i][3])}; }
            asm volatile("s_cmp_lg_u32 %24, 0\n\ts_cbranch_scc1 .Lyskip%=\n\ts_waitcnt lgkmcnt(0)\n\ts_barrier\n\t"
                         XF1("%0", "%8", "%16") XF1("%1", "%9", "%17") XF1("%2", "%10", "%18") XF1("%3", "%11", "%19")
                         XF1("%4", "%12", "%20") XF1("%5", "%13", "%21") XF1("%6", "%14", "%22") XF1("%7", "%15", "%23")
                         XF2("%0", "%8", "%16") XF2("%1", "%9", "%17") XF2("%2", "%10", "%18") XF2("%3", "%11", "%19")
                         XF2("%4", "%12", "%20") XF2("%5", "%13", "%21") XF2("%6", "%14", "%22") XF2("%7", "%15", "%23")
                         "\n.Lyskip%=:"
                         : "+v"(a[0]), "+v"(a[1]), "+v"(a[2]), "+v"(a[3]), "+v"(a[4]), "+v"(a[5]), "+v"(a[6]), "+v"(a[7])
                         : "v"(zc[0]), "v"(zc[1]), "v"(zc[2]), "v"(zc[3]), "v"(zc[4]), "v"(zc[5]), "v"(zc[6]), "v"(zc[7]),
                           "v"(g2[0]), "v"(g2[1]), "v"(g2[2]), "v"(g2[3]), "v"(g2[4]), "v"(g2[5]), "v"(g2[6]), "v"(g2[7]), "s"(k) : "memory", "scc");
            if (w2) {
                float mx = 0.f;
#pragma unroll
                for (int i = 0; i < 8; ++i) mx = fmaxf(mx, fmaxf(fabsf(fw[i].x), fabsf(fw[i].y)));
#define XM(x, y) "v_max3_f32 %0, |" x "|, |" y "|, %0\n\t"
                asm volatile("s_cmp_lg_u32 %17, 0\n\ts_cbranch_scc1 .Lzskip%=\n\t"
                             XM("%1", "%2") XM("%3", "%4") XM("%5", "%6") XM("%7", "%8") XM("%9", "%10") XM("%11", "%12") XM("%13", "%14") XM("%15", "%16")
                             "v_add_f32 %0, %0, %0\n.Lzskip%=:"
                             : "+v"(mx) : "v"(g2[0].x), "v"(g2[0].y), "v"(g2[1].x), "v"(g2[1].y), "v"(g2[2].x), "v"(g2[2].y), "v"(g2[3].x), "v"(g2[3].y),
                               "v"(g2[4].x), "v"(g2[4].y), "v"(g2[5].x), "v"(g2[5].y), "v"(g2[6].x), "v"(g2[6].y), "v"(g2[7].x), "v"(g2[7].y), "s"(k) : "scc");
                unsigned e = __float_as_uint(mx) >> 23;
                e = max(e, (unsigned)__builtin_amdgcn_mov_dpp(e, 0xB1, 0xf, 0xf, true));
                e = max(e, (unsigned)__builtin_amdgcn_mov_dpp(e, 0x4E, 0xf, 0xf, true));
                e = max(e, (unsigned)__builtin_amdgcn_mov_dpp(e, 0x141, 0xf, 0xf, true));
                e = max(e, (unsigned)__builtin_amdgcn_mov_dpp(e, 0x140, 0xf, 0xf, true));
                if ((tid & 15) == 0) LE[tid >> 4] = e;
            }
        }
    }
    STAMP(4);
    fft4<true>(a[0], a[1], a[2], a[3]);
    fft4<true>(a[4], a[5], a[6], a[7]);
#pragma unroll
    for (int i = 0; i < 4; ++i) {
        f4 v; v.xy = a[2 * i]; v.zw = a[2 * i + 1];
        *reinterpret_cast<f4*>(&L[c1 * 34 + a4 * 8 + 2 * i]) = v;
    }
    WAVE_SYNC();
#pragma unroll
    for (int c = 0; c < 8; ++c) a[c] = L[c1 * 34 + c * 4 + a4];
    WAVE_SYNC();
    cmul8<true, 1>(a, w2t);
    fft8<true>(a);
#pragma unroll
    for (int m = 0; m < 8; ++m) L[c1 * 36 + a4 + 4 * m] = a[m];
    BAR();
#pragma unroll
    for (int c = 0; c < 8; ++c) a[c] = LA[c * 36];
    asm volatile("" : "+s"(sc0));
    unsigned isc = sc0;
    if (w2) {
        unsigned em = 0;
#pragma unroll
        for (int i = 0; i < 4; ++i) em = umax5(em, reinterpret_cast<const u4*>(LE)[i]);
        isc = (min(em, 240u) + 12) << 23;
        if (tid == 0) reinterpret_cast<unsigned*>(ws + OFF_SC)[k] = isc;
    }
    const float sc = __uint_as_float(0x7f000000u - isc);
    cmul8<true, 1>(a, w1t);
    fft8<true>(a);
    STAMP(5);
#pragma unroll
    for (int m = 0; m < 8; ++m) T[256 * m] = pack_h(a[m], sc);
    VMW(8); TIE(cw); TIE(cw2);
    STAMP_END(kid);
}

__device__ __forceinline__ void cnt_add(u8* ws, int slot) {
    GAS unsigned* cnt = (GAS unsigned*)(ws + OFF_CNT + (size_t)slot * 128);
    if ((threadIdx.x & 63) == 0) __hip_atomic_fetch_add(cnt, 1u, __ATOMIC_RELAXED, __HIP_MEMORY_SCOPE_AGENT);
}
__device__ __forceinline__ void cnt_wait(u8* ws, int slot, unsigned want) {
    GAS unsigned* cnt = (GAS unsigned*)(ws + OFF_CNT + (size_t)slot * 128);
#pragma unroll 1
    for (int sp = 0; sp < SPIN; ++sp) {
        if (__builtin_amdgcn_readfirstlane(__hip_atomic_load(cnt, __ATOMIC_RELAXED, __HIP_MEMORY_SCOPE_AGENT)) >= want) break;
        __builtin_amdgcn_s_sleep(1);
    }
}
__device__ __forceinline__ void verify_frags(const u8* base, unsigned l16, int n, int jump_at, int jump) {
#pragma unroll 1
    for (int sp = 0; sp < SPIN; ++sp) {
        unsigned mx = 0;
#pragma unroll 1
        for (int i = 0; i < n; ++i) {
            u4 v;
            const unsigned o = l16 + i * 1024 + (i >= jump_at ? jump : 0);
            GLD4C(v, o, base, 0);
            VMW(0); TIE(v);
            mx = umax5(mx, v);
        }
        if (!any_mark(mx)) break;
    }
}
constexpr int LDS_STG = 8 * XR * 8, LDS_W1 = LDS_STG + 16384;
constexpr float GK0 = -2.0f * 0.7978845608028654f * 1.4426950408889634f;
constexpr float GK1 = GK0 * 0.044715f;

__global__ __launch_bounds__(256, 1) void k_fused(const u16* __restrict__ xin, u8* __restrict__ ws, const u16* __restrict__ b1,
                                                   const u16* __restrict__ b2, u16* __restrict__ out, int layer) {
    __shared__ __attribute__((aligned(16))) u8 smem[LDS_W1 + 112 * 1024];
    STAMP_DECL STAMP(0);
    const int tid = threadIdx.x, lane = tid & 63, j = tid >> 5, t = tid & 31;
    const int w = __builtin_amdgcn_readfirstlane(tid >> 6);
    const int xcd = blockIdx.x & 7, idx = blockIdx.x >> 3, s0 = xcd * 32 + idx;
    f2 tws[8];
    {
        f4 ts4[4];
#pragma unroll
        for (int i = 0; i < 4; ++i) ts4[i] = reinterpret_cast<const f4*>(TWS)[s0 * 4 + i];
        split_tw(ts4, tws);
    }
    const int xl = xcd & ((1 << OPTM) - 1);
    const int panel = (xcd >> OPTM) * (2 << OPTM) + (idx >> (4 - OPTM)), c = xl * (16 >> OPTM) + (idx & ((16 >> OPTM) - 1));
    const int rb = 2 * panel + ((idx >> (3 - OPTM)) & 1), cb = xl * (8 >> OPTM) + (idx & ((8 >> OPTM) - 1));
    const int g = lane >> 4;
    const unsigned l16 = lane * 16;
    f2* const LZ = reinterpret_cast<f2*>(smem);
    const u8* const wsrc = ws + OFF_W1T + (size_t)c * 112 * 1024 + w * 28672 + 4096;
    const unsigned lb = (unsigned)(size_t)(__attribute__((address_space(3))) u8*)smem + LDS_W1 + w * 28672 + 4096;
    const u8* const wb = ws + OFF_W2T + ((size_t)(2 * cb) * 112 + 28 * w) * 1024;
    u4 wa[28][2];
#define WL(s, a) do { GLD4A(wa[s][a], l16, wb + (a) * 112 * 1024 + ((s) >> 3) * 8192 + 4096, ((s) & 7) * 1024 - 4096); } while (0)
#define PF4(q) asm volatile("s_mov_b32 m0, %2\n\ts_nop 0\n\tglobal_load_lds_dwordx4 %0, %1 offset:%3\n\tglobal_load_lds_dwordx4 %0, %1 offset:%4\n\t" \
        "global_load_lds_dwordx4 %0, %1 offset:%5\n\tglobal_load_lds_dwordx4 %0, %1 offset:%6" :: "v"(l16), "s"(wsrc + ((q) >> 1) * 8192), "s"(lb + ((q) >> 1) * 8192), \
        "i"(((q) & 1) * 4096 - 4096), "i"(((q) & 1) * 4096 - 3072), "i"(((q) & 1) * 4096 - 2048), "i"(((q) & 1) * 4096 - 1024) : "memory")
#define WLN(i0, n) do { _Pragma("unroll") for (int i_ = (i0); i_ < (i0) + (n); ++i_) WL(i_ >> 1, i_ & 1); } while (0)
    const u4 one = {~0u, ~0u, ~0u, ~0u};
    const u8* const hcl = ws + OFF_H + ((size_t)((s0 >> 4) * 4 + w) * 112 + (s0 & 15) * 7 + 4) * 1024;
    const u8* const xcl = ws + OFF_X2 + (size_t)(8 * s0 + 2 * w) * 1024;
#define CLR() do { GST4C(one, l16, hcl, 0); GST4C(one, l16, hcl, 1024); if (!OPTX2 && layer == 0) { GST4C(one, l16, xcl, 0); GST4C(one, l16, xcl, 1024); } } while (0)
#define POLL(off, base) do { _Pragma("unroll 1") for (int sp = 0; sp < SPIN; ++sp) { unsigned pv_; GLD1C(pv_, off, base, 0); VMW(0); TIE(pv_); if (!any_mark(pv_)) break; } } while (0)
    u4 tq[2];
    float tsc;
    unsigned raw[8];
    u4 bias[3];
    u2 bias3;
    {
        const u8* tb = ws + OFF_T + (size_t)s0 * 32 + (size_t)w * 64 * 8192;
        const unsigned to = (lane & 62) * 8192u + (lane & 1) * 16, t4 = tid * 4;
        GLD4(tq[0], to, tb, 0);
        GLD4(tq[1], to, tb + 8192, 0);
        GLD1(tsc, t4, ws + OFF_SC, 0);
    }
    f2 w1t[8], w2t[8], wp[8];
    tw8(t * (1.f / 256), w1t); tw8((t & 3) * (1.f / 32), w2t);
    {
        const float R = 0.70710678118654752440f;
        const float x0 = (16 * (t & 3) + (t >> 2)) * (1.f / 512);
        wp[0] = cis(x0);
        wp[4] = cis(x0 + 1.f / 64);
#pragma unroll
        for (int b = 0; b < 2; ++b) {
            const f2 q = wp[4 * b];
            wp[4 * b + 1] = add_mi(q, q) * R;
            wp[4 * b + 2] = f2{q.y, -q.x};
            wp[4 * b + 3] = add_pi(q, q) * (-R);
        }
    }
#pragma unroll
    for (int i = 0; i < 8; ++i) { TIE(w1t[i]); TIE(w2t[i]); TIE(wp[i]); }
    {
        const unsigned xo = (unsigned)(layer ? 8 * s0 + j : s0 + 256 * j) * 1024u + t * 4;
#pragma unroll
        for (int m = 0; m < 8; ++m) GLD1(raw[m], xo, xin, 128 * m);
        PF4(0); PF4(1);
    }
    VMW(16);
    TIE(tq[0]); TIE(tq[1]); TIE(tsc);
    STAMP(1);
    {
        f2 a[8];
        {
            u4 lo, hi;
            pair_swap(tq[0], tq[1], lo, hi, lane & 1);
#pragma unroll
            for (int q = 0; q < 4; ++q) { a[q] = f2{mixl0(lo[q], tsc), mixh0(lo[q], tsc)}; a[4 + q] = f2{mixl0(hi[q], tsc), mixh0(hi[q], tsc)}; }
        }
        cmul8s<true>(a, tws);
        fft8<true>(a);
        {
            f2* Z = LZ + zphys(tid);
#pragma unroll
            for (int jj = 0; jj < 8; ++jj) Z[jj * XR] = a[jj];
        }
        CLR(); PF4(2);
        BAR();
        f2* L = LZ + j * XR;
        const int c1 = t >> 2, a4 = t & 3;
        {
            const f2* Yk = L + 20 * a4 + c1;
            const f2* Ym0 = L + 76 - 20 * a4 - c1 + (c1 == 0 ? 4 : 0);
            const f2* Ym1 = L + 68 - 20 * a4 - c1;
            f2 sm[8], df[8], r[8];
#pragma unroll
            for (int i = 0; i < 8; ++i) {
                const f2 yk = Yk[80 * (i & 3) + 8 * (i >> 2)], ym = ((i >> 2) ? Ym1 : Ym0)[80 * (3 - (i & 3))];
                sm[i] = add_cj(yk, ym); df[i] = sub_cj(yk, ym);
                if (i == 0 && t == 0) { sm[0] = f2{yk.x + yk.y, yk.x - yk.y}; df[0] = f2{0.f, 0.f}; }
            }
#pragma unroll
            for (int i = 0; i < 8; ++i) r[i] = cm_a(df[i], wp[i]);
#pragma unroll
            for (int i = 0; i < 8; ++i) r[i] = cm_b<true>(df[i], wp[i], r[i]);
#pragma unroll
            for (int i = 0; i < 8; ++i) a[i] = add_pi(sm[i], r[i]);
        }
        WAVE_SYNC();
        fft4<true>(a[0], a[1], a[2], a[3]);
        fft4<true>(a[4], a[5], a[6], a[7]);
#pragma unroll
        for (int i = 0; i < 4; ++i) {
            f4 v; v.xy = a[2 * i]; v.zw = a[2 * i + 1];
            *reinterpret_cast<f4*>(&L[c1 * 34 + a4 * 8 + 2 * i]) = v;
        }
        WAVE_SYNC();
#pragma unroll
        for (int cc = 0; cc < 8; ++cc) a[cc] = L[c1 * 34 + cc * 4 + a4];
        PF4(3);
        WAVE_SYNC();
        cmul8<true, 1>(a, w2t);
        fft8<true>(a);
#pragma unroll
        for (int m = 0; m < 8; ++m) L[c1 * 36 + a4 + 4 * m] = a[m];
        WAVE_SYNC();
#pragma unroll
        for (int cc = 0; cc < 8; ++cc) a[cc] = L[cc * 36 + t];
        PF4(4);
        WAVE_SYNC();
        cmul8<true, 1>(a, w1t);
        fft8<true>(a);
        SB();
        VMW(20 + 2);
#pragma unroll
        for (int m = 0; m < 8; ++m) TIE(raw[m]);
        PF4(5);
        SB();
        const float SC = 1.0f / 2097152.0f;
        unsigned* st = reinterpret_cast<unsigned*>(L) + (j & 3) * 16;
#pragma unroll
        for (int m = 0; m < 8; ++m) {
            const f2 ys = a[m] * SC;
            const unsigned y = pack_bf16(ys.x, ys.y);
            const f2 xs = f2{bflo(raw[m]), bfhi(raw[m])} + f2{bflo(y), bfhi(y)};
            const unsigned x1 = pack_bf16(xs.x, xs.y);
            st[256 + t + 32 * m] = x1;
            a[m] = f2{bflo(x1), bfhi(x1)};
        }
        SB();
        PF4(6);
        {
            const unsigned bo = g * 56;
            const u16* bb = b1 + 112 * c;
#pragma unroll
            for (int n = 0; n < 3; ++n) GLD4(bias[n], bo, bb, 16 * n);
            GLD2(bias3, bo, bb, 48);
        }
        WLN(0, WA0);
        SB();
        unsigned xn[8];
        layernorm16(a, xn);
#pragma unroll
        for (int m = 0; m < 8; ++m) st[t + 32 * m] = nomark(xn[m]);
        VMW(20 + 4 + WA0);
    }
    BAR();
    STAMP(2);
    {
        const int row = lane & 7, kq = (lane >> 3) & 3, kl = lane >> 5;
        const u8* src = smem + row * (XR * 8) + (row & 3) * 64 + kl * 64 + kq * 16 + w * 256;
        const u8* xnb = ws + OFF_XN + (size_t)(s0 >> 1) * 16384 + (s0 & 1) * 128 + w * 4096;
        const unsigned xo = kl * 1024 + kq * 256 + row * 16;
        u4 v[2], z[2];
#pragma unroll
        for (int i = 0; i < 2; ++i) v[i] = *reinterpret_cast<const u4*>(src + i * 128);
#pragma unroll
        for (int i = 0; i < 2; ++i) z[i] = *reinterpret_cast<const u4*>(smem + (2 * w + i) * (XR * 8) + ((2 * w + i) & 3) * 64 + 1024 + l16);
#pragma unroll
        for (int i = 0; i < 2; ++i) GST4C(v[i], xo, xnb, i * 2048);
        const u8* x1b = ws + OFF_X1 + (size_t)s0 * 8192 + w * 2048;
#pragma unroll
        for (int i = 0; i < 2; ++i) GST4C(z[i], l16, x1b, i * 1024);
        WLN(WA0, WA1);
        VMW(WA1);
        cnt_add(ws, layer * 32 + (s0 >> 3));
    }
    STAMP(3);
    BAR();
    STAMP(4);
    unsigned o[2][14];
    const u8* hb = ws + OFF_H + ((size_t)((panel * 2 + (w >> 1)) * 2) * 112 + c * 7) * 1024;
    const unsigned ho = (unsigned)(g * 512 + ((w & 1) * 16 + (lane & 15)) * 16);
    {
        u4 xf[2][16];
        f4 acc[2][7], bf[7];
        bf16x8 af[6];
        const u8* xb = ws + OFF_XN + (size_t)(panel * 8 + 2 * w) * 16384;
        const unsigned ho2 = (unsigned)(6 * 1024 + (g >> 1) * 512 + (g & 1) * 8 + ((w & 1) * 16 + (lane & 15)) * 16);
#define XL(tt, ks) GLD4C(xf[tt][ks], l16, xb + (tt) * 16384 + ((ks) >> 3) * 8192 + 4096, ((ks) & 7) * 1024 - 4096)
#define XW(ks, N) do { VMW(N); TIE(xf[0][ks]); TIE(xf[1][ks]); } while (0)
        TIE(bias[0]); TIE(bias[1]); TIE(bias[2]); TIE(bias3);
#pragma unroll
        for (int n = 0; n < 7; ++n) {
            const unsigned p0 = n < 6 ? bias[n >> 1][2 * (n & 1)] : bias3[0], p1 = n < 6 ? bias[n >> 1][2 * (n & 1) + 1] : bias3[1];
            bf[n] = f4{bflo(p0), bfhi(p0), bflo(p1), bfhi(p1)}; TIE(bf[n]);
        }
        const unsigned xpo = ((lane >> 3) & 1) * 16384 + ((lane & 3) * 4 + 3) * 1024 + 768 + ((lane >> 2) & 1) * 128 + 124;
        typedef __attribute__((address_space(3))) const bf16x8* lds_p;
        lds_p la0 = (lds_p)(smem + LDS_W1 + l16), la1 = la0 + 56 * 64;
        TIE(la0); TIE(la1);
#define RD(sl, ks, n) af[sl] = ((ks) < 8 ? la0 : la1)[(((ks) & 7) * 7 + (n)) * 64]
#define MF0(tt, n, ks, sl) asm volatile("v_mfma_f32_16x16x32_bf16 %0, %1, %2, %3" : "=&v"(acc[tt][n]) : "v"(af[sl]), "v"(xf[tt][ks]), "v"(bf[n]))
#define MF(tt, n, ks, sl) asm volatile("v_mfma_f32_16x16x32_bf16 %0, %1, %2, %0" : "+v"(acc[tt][n]) : "v"(af[sl]), "v"(xf[tt][ks]))
#define NOPS(n) asm volatile("s_nop %0" :: "i"(n))
#define GP(tt, u) do { const f2 x_ = f2{acc[tt][(u) >> 1][2 * ((u) & 1)], acc[tt][(u) >> 1][2 * ((u) & 1) + 1]}; \
            const f2 q_ = __builtin_elementwise_fma(x_ * x_, f2{GK1, GK1}, f2{GK0, GK0}) * x_; \
            const f2 e_ = f2{__builtin_amdgcn_exp2f(q_.x), __builtin_amdgcn_exp2f(q_.y)} + 1.0f; \
            const f2 y_ = x_ * f2{__builtin_amdgcn_rcpf(e_.x), __builtin_amdgcn_rcpf(e_.y)}; \
            const unsigned p_ = pack_bf16(y_.x, y_.y); o[tt][u] = ((u) >> 2) == 2 ? nomark(p_) : p_; } while (0)
#define HS(tt, ch) do { const u4 v_ = {o[tt][4 * (ch)], o[tt][4 * (ch) + 1], o[tt][4 * (ch) + 2], o[tt][4 * (ch) + 3]}; \
            GST4C(v_, ho, hb + (tt) * 112 * 1024 + 2048, (ch) * 2048 - 2048); } while (0)
#define HS2(tt) do { const u2 v_ = {o[tt][12], o[tt][13]}; GST2C(v_, ho2, hb + (tt) * 112 * 1024, 0); } while (0)
bool ver = false;
#pragma unroll 1
        for (;;) {
        RD(0,0,0); RD(1,0,1); RD(2,0,2); POLL(xpo, xb); STAMP(8); XL(0,0); XL(1,0); XL(0,1); XL(1,1); XL(0,2); XL(1,2); XL(0,3); XL(1,3); XL(0,4);
        XL(1,4); XL(0,5); XL(1,5); SB(); XW(0,10); RD(3,0,3); MF0(0,0,0,0); XL(0,6); MF0(1,0,0,0); SB(); RD(4,0,4); MF0(0,1,0,1); XL(1,6);
        MF0(1,1,0,1); SB(); RD(5,0,5); MF0(0,2,0,2); WL(4,0); MF0(1,2,0,2); SB(); RD(0,0,6); MF0(0,3,0,3); MF0(1,3,0,3); SB(); RD(1,1,0);
        MF0(0,4,0,4); WL(4,1); MF0(1,4,0,4); SB(); RD(2,1,1); MF0(0,5,0,5); MF0(1,5,0,5); SB(); RD(3,1,2); MF0(0,6,0,0); MF0(1,6,0,0); SB(); XW(1,12);
        RD(4,1,3); MF(0,0,1,1); XL(0,7); MF(1,0,1,1); SB(); RD(5,1,4); MF(0,1,1,2); XL(1,7); MF(1,1,1,2); SB(); RD(0,1,5); MF(0,2,1,3); WL(5,0);
        MF(1,2,1,3); SB(); RD(1,1,6); MF(0,3,1,4); MF(1,3,1,4); SB(); RD(2,2,0); MF(0,4,1,5); WL(5,1); MF(1,4,1,5); SB(); RD(3,2,1); MF(0,5,1,0);
        MF(1,5,1,0); SB(); RD(4,2,2); MF(0,6,1,1); MF(1,6,1,1); SB(); XW(2,14); RD(5,2,3); MF(0,0,2,2); XL(0,8); MF(1,0,2,2); SB(); RD(0,2,4);
        MF(0,1,2,3); XL(1,8); MF(1,1,2,3); SB(); RD(1,2,5); MF(0,2,2,4); WL(6,0); MF(1,2,2,4); SB(); RD(2,2,6); MF(0,3,2,5); MF(1,3,2,5); SB();
        RD(3,3,0); MF(0,4,2,0); WL(6,1); MF(1,4,2,0); SB(); RD(4,3,1); MF(0,5,2,1); MF(1,5,2,1); SB(); RD(5,3,2); MF(0,6,2,2); MF(1,6,2,2); SB();
        XW(3,16); RD(0,3,3); MF(0,0,3,3); XL(0,9); MF(1,0,3,3); SB(); RD(1,3,4); MF(0,1,3,4); XL(1,9); MF(1,1,3,4); SB(); RD(2,3,5); MF(0,2,3,5);
        WL(7,0); MF(1,2,3,5); SB(); RD(3,3,6); MF(0,3,3,0); MF(1,3,3,0); SB(); RD(4,4,0); MF(0,4,3,1); WL(7,1); MF(1,4,3,1); SB(); RD(5,4,1);
        MF(0,5,3,2); MF(1,5,3,2); SB(); RD(0,4,2); MF(0,6,3,3); MF(1,6,3,3); SB(); XW(4,18); RD(1,4,3); MF(0,0,4,4); XL(0,10); MF(1,0,4,4); SB();
        RD(2,4,4); MF(0,1,4,5); XL(1,10); MF(1,1,4,5); SB(); RD(3,4,5); MF(0,2,4,0); WL(8,0); MF(1,2,4,0); SB(); RD(4,4,6); MF(0,3,4,1); MF(1,3,4,1);
        SB(); RD(5,5,0); MF(0,4,4,2); WL(8,1); MF(1,4,4,2); SB(); RD(0,5,1); MF(0,5,4,3); MF(1,5,4,3); SB(); RD(1,5,2); MF(0,6,4,4); MF(1,6,4,4);
        SB(); XW(5,20); RD(2,5,3); MF(0,0,5,5); XL(0,11); MF(1,0,5,5); SB(); RD(3,5,4); MF(0,1,5,0); XL(1,11); MF(1,1,5,0); SB(); RD(4,5,5);
        MF(0,2,5,1); WL(9,0); MF(1,2,5,1); SB(); RD(5,5,6); MF(0,3,5,2); MF(1,3,5,2); SB(); RD(0,6,0); MF(0,4,5,3); WL(9,1); MF(1,4,5,3); SB();
        RD(1,6,1); MF(0,5,5,4); MF(1,5,5,4); SB(); RD(2,6,2); MF(0,6,5,5); MF(1,6,5,5); SB(); XW(6,22); RD(3,6,3); MF(0,0,6,0); XL(0,12); MF(1,0,6,0);
        SB(); RD(4,6,4); MF(0,1,6,1); XL(1,12); MF(1,1,6,1); SB(); RD(5,6,5); MF(0,2,6,2); WL(10,0); MF(1,2,6,2); SB(); RD(0,6,6); MF(0,3,6,3);
        MF(1,3,6,3); SB(); RD(1,7,0); MF(0,4,6,4); WL(10,1); MF(1,4,6,4); SB(); RD(2,7,1); MF(0,5,6,5); MF(1,5,6,5); SB(); RD(3,7,2); MF(0,6,6,0);
        MF(1,6,6,0); SB(); XW(7,22); RD(4,7,3); MF(0,0,7,1); XL(0,13); MF(1,0,7,1); SB(); RD(5,7,4); MF(0,1,7,2); XL(1,13); MF(1,1,7,2); SB();
        RD(0,7,5); MF(0,2,7,3); WL(11,0); MF(1,2,7,3); SB(); RD(1,7,6); MF(0,3,7,4); MF(1,3,7,4); SB(); RD(2,8,0); MF(0,4,7,5); WL(11,1); MF(1,4,7,5);
        SB(); RD(3,8,1); MF(0,5,7,0); MF(1,5,7,0); SB(); RD(4,8,2); MF(0,6,7,1); MF(1,6,7,1); SB(); XW(8,22); RD(5,8,3); MF(0,0,8,2); XL(0,14);
        MF(1,0,8,2); SB(); RD(0,8,4); MF(0,1,8,3); XL(1,14); MF(1,1,8,3); SB(); RD(1,8,5); MF(0,2,8,4); WL(12,0); MF(1,2,8,4); SB(); RD(2,8,6);
        MF(0,3,8,5); MF(1,3,8,5); SB(); RD(3,9,0); MF(0,4,8,0); WL(12,1); MF(1,4,8,0); SB(); RD(4,9,1); MF(0,5,8,1); MF(1,5,8,1); SB(); RD(5,9,2);
        MF(0,6,8,2); MF(1,6,8,2); SB(); XW(9,22); RD(0,9,3); MF(0,0,9,3); XL(0,15); MF(1,0,9,3); SB(); RD(1,9,4); MF(0,1,9,4); XL(1,15); MF(1,1,9,4);
        SB(); RD(2,9,5); MF(0,2,9,5); WL(13,0); MF(1,2,9,5); SB(); RD(3,9,6); MF(0,3,9,0); MF(1,3,9,0); SB(); RD(4,10,0); MF(0,4,9,1); WL(13,1);
        MF(1,4,9,1); SB(); RD(5,10,1); MF(0,5,9,2); MF(1,5,9,2); SB(); RD(0,10,2); MF(0,6,9,3); MF(1,6,9,3); SB(); XW(10,22); RD(1,10,3);
        MF(0,0,10,4); WL(14,0); MF(1,0,10,4); SB(); RD(2,10,4); MF(0,1,10,5); WL(14,1); MF(1,1,10,5); SB(); RD(3,10,5); MF(0,2,10,0); WL(15,0);
        MF(1,2,10,0); SB(); RD(4,10,6); MF(0,3,10,1); MF(1,3,10,1); SB(); RD(5,11,0); MF(0,4,10,2); WL(15,1); MF(1,4,10,2); SB(); RD(0,11,1);
        MF(0,5,10,3); MF(1,5,10,3); SB(); RD(1,11,2); MF(0,6,10,4); MF(1,6,10,4); SB(); XW(11,22); RD(2,11,3); MF(0,0,11,5); WL(16,0); MF(1,0,11,5);
        SB(); RD(3,11,4); MF(0,1,11,0); WL(16,1); MF(1,1,11,0); SB(); RD(4,11,5); MF(0,2,11,1); WL(17,0); MF(1,2,11,1); SB(); RD(5,11,6);
        MF(0,3,11,2); MF(1,3,11,2); SB(); RD(0,12,0); MF(0,4,11,3); WL(17,1); MF(1,4,11,3); SB(); RD(1,12,1); MF(0,5,11,4); MF(1,5,11,4); SB();
        RD(2,12,2); MF(0,6,11,5); MF(1,6,11,5); SB(); XW(12,22); RD(3,12,3); MF(0,0,12,0); WL(18,0); MF(1,0,12,0); SB(); RD(4,12,4); MF(0,1,12,1);
        WL(18,1); MF(1,1,12,1); SB(); RD(5,12,5); MF(0,2,12,2); WL(19,0); MF(1,2,12,2); SB(); RD(0,12,6); MF(0,3,12,3); MF(1,3,12,3); SB();
        RD(1,13,0); MF(0,4,12,4); WL(19,1); MF(1,4,12,4); SB(); RD(2,13,1); MF(0,5,12,5); MF(1,5,12,5); SB(); RD(3,13,2); MF(0,6,12,0); MF(1,6,12,0);
        SB(); XW(13,22); RD(4,13,3); MF(0,0,13,1); WL(20,0); MF(1,0,13,1); SB(); RD(5,13,4); MF(0,1,13,2); WL(20,1); MF(1,1,13,2); SB(); RD(0,13,5);
        MF(0,2,13,3); WL(21,0); MF(1,2,13,3); SB(); RD(1,13,6); MF(0,3,13,4); MF(1,3,13,4); SB(); RD(2,14,0); MF(0,4,13,5); WL(21,1); MF(1,4,13,5);
        SB(); RD(3,14,1); MF(0,5,13,0); MF(1,5,13,0); SB(); RD(4,14,2); MF(0,6,13,1); MF(1,6,13,1); SB(); XW(14,22); RD(5,14,3); MF(0,0,14,2);
        WL(22,0); MF(1,0,14,2); SB(); RD(0,14,4); MF(0,1,14,3); WL(22,1); MF(1,1,14,3); SB(); RD(1,14,5); MF(0,2,14,4); WL(23,0); MF(1,2,14,4); SB();
        RD(2,14,6); MF(0,3,14,5); MF(1,3,14,5); SB(); RD(3,15,0); MF(0,4,14,0); WL(23,1); MF(1,4,14,0); SB(); RD(4,15,1); MF(0,5,14,1); MF(1,5,14,1);
        SB(); RD(5,15,2); MF(0,6,14,2); MF(1,6,14,2); SB(); XW(15,22); RD(0,15,3); MF(0,0,15,3); MF(1,0,15,3); SB(); RD(1,15,4); MF(0,1,15,4);
        MF(1,1,15,4); SB(); RD(2,15,5); MF(0,2,15,5); MF(1,2,15,5); SB(); RD(3,15,6); MF(0,3,15,0); MF(1,3,15,0); SB(); MF(0,4,15,1); MF(1,4,15,1);
        SB(); MF(0,5,15,2); MF(1,5,15,2); SB(); MF(0,6,15,3); MF(1,6,15,3); SB(); NOPS(7); SB();
        if (ver || !any_nan(acc[0][0][0] + acc[1][0][0])) break;
        verify_frags(xb, l16, 32, 32, 0); ver = true;
        }
        STAMP(5); GP(0,12); GP(0,13); HS2(0); WL(24,0); WL(24,1); WL(25,0); GP(0,0); GP(0,1); GP(0,2); GP(0,3); HS(0,0); WL(25,1); WL(26,0); WL(26,1);
        GP(0,4); GP(0,5); GP(0,6); GP(0,7); HS(0,1); WL(27,0); WL(27,1); GP(0,8); GP(0,9); GP(0,10); GP(0,11); GP(1,12); GP(1,13); HS2(1); GP(1,0);
        GP(1,1); GP(1,2); GP(1,3); HS(1,0); SB(); STAMP(13); VMW(4); HS(0,2); GP(1,4); GP(1,5); GP(1,6); GP(1,7); HS(1,1); GP(1,8); GP(1,9); GP(1,10);
        GP(1,11); TIE(o[1][8]); TIE(o[1][9]); TIE(o[1][10]); TIE(o[1][11]);
    }
    STAMP(6);
    BAR();
    STAMP(7);
    {
        const int tk = tid >> 3, dq = tid & 7;
        const int rp = rb * 64 + (tk >> 4) * 32 + (tk & 15);
        u4 hf[28][2], xr[2], bb;
        f16v acc[2][2];
        const u8* hsrc = ws + OFF_H + ((size_t)(2 * rb) * 112 + 28 * w) * 1024;
        const u8* cntp = ws + OFF_CNT + (size_t)(layer * 32 + rb) * 128;
        const unsigned zero = 0;
        const unsigned hpo = ((lane & 3) * 7 + 5) * 1024 + 512 + ((lane >> 2) & 1) * 256 + 252;
        unsigned pv0, pv1;
        unsigned cnt;
        const unsigned xro = rp * 1024 + dq * 16, bo2 = dq * 16;
        const u8* x1b = ws + OFF_X1 + cb * 128;
        const u16* b2b = b2 + cb * 64;
        float* red = reinterpret_cast<float*>(smem + LDS_W1);
        float* rw = red + w * (64 * 68) + (lane & 31) * 68 + 4 * (lane >> 5);
#define HL(s, b) GLD4C(hf[s][b], l16, hsrc + (b) * 112 * 1024 + ((s) >> 3) * 8192 + 4096, ((s) & 7) * 1024 - 4096)
#define CL() GLD1C(cnt, zero, cntp, 0)
#define EL() do { TIE(cnt); if (__builtin_amdgcn_readfirstlane(cnt) < 32u) cnt_wait(ws, layer * 32 + rb, 32u); GLD4C(xr[0], xro, x1b, 0); GLD4C(xr[1], xro, x1b + 16384, 0); GLD4(bb, bo2, b2b, 0); } while (0)
#define HW(N) VMW(N)
#define M20(s, a, b) asm volatile("v_mfma_f32_32x32x16_bf16 %0, %1, %2, 0" : "=&v"(acc[a][b]) : "a"(wa[s][a]), "v"(hf[s][b]))
#define M2(s, a, b) asm volatile("v_mfma_f32_32x32x16_bf16 %0, %1, %2, %0" : "+v"(acc[a][b]) : "a"(wa[s][a]), "v"(hf[s][b]))
#define RW(a, b, rq) do { const f4 v_ = {acc[a][b][4 * (rq)], acc[a][b][4 * (rq) + 1], acc[a][b][4 * (rq) + 2], acc[a][b][4 * (rq) + 3]}; \
            *reinterpret_cast<f4*>(rw + 32 * (b) * 68 + 32 * (a) + 8 * (rq)) = v_; } while (0)
        GLD1C(pv0, hpo, hsrc, 0); VMW(1); HS(1,2); VMW(1); TIE(pv0);
#pragma unroll 1
        for (int rp_ = 0; rp_ < SPIN; ++rp_) {
        if (any_mark(pv0)) POLL(hpo, hsrc); STAMP(11); unsigned mx = 0; HL(0,0); HL(1,0); HL(2,0); HL(3,0); HL(6,0); HL(7,0); HL(8,0); HL(9,0);
        HL(10,0); HL(13,0); HL(14,0); HL(15,0); SB(); HW(11); M20(0,0,0); HL(16,0); M20(0,1,0); SB(); HW(11); M2(1,0,0); HL(17,0); M2(1,1,0); SB();
        HW(11); M2(2,0,0); HL(20,0); M2(2,1,0); SB(); HW(11); M2(3,0,0); HL(21,0); GLD1C(pv1, hpo, hsrc + 112 * 1024, 0); M2(3,1,0); SB(); HW(12);
        M2(6,0,0); HL(22,0); M2(6,1,0); SB(); HW(12); M2(7,0,0); HL(23,0); M2(7,1,0); SB(); HW(12); M2(8,0,0); HL(24,0); M2(8,1,0); SB(); HW(12);
        M2(9,0,0); HL(27,0); M2(9,1,0); SB(); HW(12); M2(10,0,0); HL(4,0); CL(); M2(10,1,0); SB(); HW(13); M2(13,0,0); HL(5,0); M2(13,1,0); SB();
        HW(13); M2(14,0,0); HL(11,0); M2(14,1,0); SB(); HW(13); M2(15,0,0); HL(12,0); M2(15,1,0); SB(); HW(13); M2(16,0,0); HL(18,0); M2(16,1,0);
        SB(); HW(13); M2(17,0,0); HL(19,0); M2(17,1,0); SB(); HW(13); M2(20,0,0); HL(25,0); M2(20,1,0); SB(); HW(13); M2(21,0,0); HL(26,0);
        M2(21,1,0); SB(); HW(12); M2(22,0,0); TIE(pv1); if (any_mark(pv1)) POLL(hpo, hsrc + 112 * 1024); HL(0,1); M2(22,1,0); SB(); HW(12);
        M2(23,0,0); HL(1,1); M2(23,1,0); SB(); HW(12); M2(24,0,0); HL(2,1); M2(24,1,0); SB(); HW(12); M2(27,0,0); HL(3,1); M2(27,1,0); SB(); HW(12);
        M2(4,0,0); HL(6,1); M2(4,1,0); TIE(hf[4][0]); mx = umax5(mx, hf[4][0]); TIE(mx); SB(); HW(11); M2(5,0,0); HL(7,1); M2(5,1,0);
        TIE(hf[5][0]); mx = umax5(mx, hf[5][0]); TIE(mx); SB(); HW(11); M2(11,0,0); HL(8,1); M2(11,1,0);
        TIE(hf[11][0]); mx = umax5(mx, hf[11][0]); TIE(mx); SB(); HW(11); M2(12,0,0); HL(9,1); M2(12,1,0);
        TIE(hf[12][0]); mx = umax5(mx, hf[12][0]); TIE(mx); SB(); HW(11); M2(18,0,0); HL(10,1); M2(18,1,0);
        TIE(hf[18][0]); mx = umax5(mx, hf[18][0]); TIE(mx); SB(); HW(11); M2(19,0,0); HL(13,1); M2(19,1,0);
        TIE(hf[19][0]); mx = umax5(mx, hf[19][0]); TIE(mx); SB(); HW(11); M2(25,0,0); HL(14,1); M2(25,1,0);
        TIE(hf[25][0]); mx = umax5(mx, hf[25][0]); TIE(mx); SB(); HW(11); M2(26,0,0); HL(15,1); M2(26,1,0);
        TIE(hf[26][0]); mx = umax5(mx, hf[26][0]); TIE(mx); SB(); HW(11); M20(0,0,1); HL(16,1); M20(0,1,1); SB(); HW(11); M2(1,0,1); HL(17,1);
        M2(1,1,1); SB(); HW(11); M2(2,0,1); HL(20,1); M2(2,1,1); SB(); HW(11); M2(3,0,1); HL(21,1); M2(3,1,1); SB(); HW(11); M2(6,0,1); HL(22,1);
        M2(6,1,1); RW(0,0,0); SB(); HW(11); M2(7,0,1); HL(23,1); M2(7,1,1); SB(); HW(11); M2(8,0,1); HL(24,1); M2(8,1,1); RW(0,0,1); SB(); HW(11);
        M2(9,0,1); HL(27,1); M2(9,1,1); SB(); HW(11); M2(10,0,1); HL(4,1); M2(10,1,1); RW(0,0,2); SB(); HW(11); M2(13,0,1); HL(5,1); M2(13,1,1); SB();
        HW(11); M2(14,0,1); HL(11,1); M2(14,1,1); RW(0,0,3); SB(); HW(11); M2(15,0,1); HL(12,1); M2(15,1,1); SB(); HW(11); M2(16,0,1); HL(18,1);
        M2(16,1,1); RW(1,0,0); SB(); HW(11); M2(17,0,1); HL(19,1); M2(17,1,1); SB(); HW(11); M2(20,0,1); HL(25,1); M2(20,1,1); RW(1,0,1); SB();
        HW(11); M2(21,0,1); HL(26,1); M2(21,1,1); SB(); HW(11); M2(22,0,1); EL(); M2(22,1,1); RW(1,0,2); SB(); HW(13); M2(23,0,1); M2(23,1,1); SB();
        HW(12); M2(24,0,1); M2(24,1,1); RW(1,0,3); SB(); HW(11); M2(27,0,1); M2(27,1,1); SB(); HW(10); M2(4,0,1); M2(4,1,1);
        TIE(hf[4][1]); mx = umax5(mx, hf[4][1]); TIE(mx); SB(); HW(9); M2(5,0,1); M2(5,1,1); TIE(hf[5][1]); mx = umax5(mx, hf[5][1]); TIE(mx); SB();
        HW(8); M2(11,0,1); M2(11,1,1); TIE(hf[11][1]); mx = umax5(mx, hf[11][1]); TIE(mx); SB(); HW(7); M2(12,0,1); M2(12,1,1);
        TIE(hf[12][1]); mx = umax5(mx, hf[12][1]); TIE(mx); SB(); HW(6); M2(18,0,1); M2(18,1,1); TIE(hf[18][1]); mx = umax5(mx, hf[18][1]); TIE(mx);
        SB(); HW(5); M2(19,0,1); M2(19,1,1); TIE(hf[19][1]); mx = umax5(mx, hf[19][1]); TIE(mx); SB(); HW(4); M2(25,0,1); M2(25,1,1);
        TIE(hf[25][1]); mx = umax5(mx, hf[25][1]); TIE(mx); SB(); HW(3); M2(26,0,1); M2(26,1,1); TIE(hf[26][1]); mx = umax5(mx, hf[26][1]); TIE(mx);
        SB(); NOPS(7); NOPS(3); SB(); RW(0,1,0); RW(0,1,1); RW(0,1,2); RW(0,1,3); RW(1,1,0); RW(1,1,1); RW(1,1,2); RW(1,1,3);
        if (!any_mark(mx)) break;
        }
        STAMP(9);
        VMW(0);
        TIE(xr[0]); TIE(xr[1]); TIE(bb);
        BAR();
        const u8* ob = layer ? reinterpret_cast<const u8*>(out) + cb * 128 : ws + OFF_X2 + cb * 128;
#pragma unroll
        for (int b = 0; b < 2; ++b) {
            f4 sum[2];
#pragma unroll
            for (int i = 0; i < 2; ++i) {
                const float* rp0 = red + (32 * b + tk) * 68 + dq * 8 + 4 * i;
                f4 v0 = *reinterpret_cast<const f4*>(rp0), v1 = *reinterpret_cast<const f4*>(rp0 + 64 * 68);
                f4 v2 = *reinterpret_cast<const f4*>(rp0 + 2 * 64 * 68), v3 = *reinterpret_cast<const f4*>(rp0 + 3 * 64 * 68);
                sum[i] = (v0 + v1) + (v2 + v3);
            }
            u4 ov;
#pragma unroll
            for (int i = 0; i < 4; ++i) {
                const unsigned xv = xr[b][i], bv = bb[i];
                const unsigned h = pack_bf16(sum[i >> 1][2 * (i & 1)], sum[i >> 1][2 * (i & 1) + 1]);
                const f2 s2 = f2{bflo(h), bfhi(h)} + f2{bflo(bv), bfhi(bv)};
                const unsigned s = pack_bf16(s2.x, s2.y);
                const f2 r2 = f2{bflo(xv), bfhi(xv)} + f2{bflo(s), bfhi(s)};
                ov[i] = nomark(pack_bf16(r2.x, r2.y));
            }
            const int r = rp + 16 * b;
            const unsigned oo = (unsigned)(layer ? (r >> 3) + 256 * (r & 7) : r) * 1024u + dq * 16;
            GST4C(ov, oo, ob, 0);
        }
    }
    STAMP(10);
    if (layer == 0) {
        unsigned rw2[8];
        const unsigned xo = (unsigned)(8 * s0 + j) * 1024u + t * 4;
        const u8* x2b = ws + OFF_X2;
        f2 w1u[8], w2u[8];
        int t2 = t; TIE(t2);
        tw8(t2 * (1.f / 256), w1u); tw8((t2 & 3) * (1.f / 32), w2u);
        f2 w512 = cis(tid * (1.f / 512));
#pragma unroll
        for (int i = 0; i < 8; ++i) { TIE(w1u[i]); TIE(w2u[i]); }
        TIE(w512);
        VMW(0);
        STAMP(12);
#pragma unroll 1
        for (int sp = 0; sp < SPIN; ++sp) {
#pragma unroll
            for (int m = 0; m < 8; ++m) GLD1C(rw2[m], xo, x2b, 128 * m);
            VMW(0);
#pragma unroll
            for (int m = 0; m < 8; ++m) TIE(rw2[m]);
            if (!any_mark(umax3(umax3(umax3(rw2[0], rw2[1], rw2[2]), rw2[3], rw2[4]), umax3(rw2[5], rw2[6], rw2[7]), 0u))) break;
        }
        STAMP(13);
        rowfwd_body(rw2, w1u, w2u, w512, tws, LZ, smem + LDS_STG, s0, ws);
        STAMP(14);
    }
    STAMP_END(2 + 2 * layer);
}

extern "C" void kernel_launch(void* const* d_in, const int* in_sizes, int n_in,
                              void* d_out, int out_size, void* d_ws, size_t ws_size,
                              hipStream_t stream) {
    const u16* x  = (const u16*)d_in[0];
    const u16* wr = (const u16*)d_in[1];
    const u16* wi = (const u16*)d_in[2];
    const u16* w1 = (const u16*)d_in[4];
    const u16* b1 = (const u16*)d_in[5];
    const u16* w2 = (const u16*)d_in[6];
    const u16* b2 = (const u16*)d_in[7];
    u8* ws = (u8*)d_ws;
    u16* out = (u16*)d_out;
    const u16* x2 = (const u16*)(ws + OFF_X2);
    k_rowfwd<<<256 + NPREP_WF + NPREP_G + NPREP_W1, 256, 0, stream>>>(x, ws, wr, wi, w1, w2, b1, b2, out KID(0));
    k_col<<<256 + NPREP_W2, 256, 0, stream>>>(ws, w2, w1 KID(1));
    k_fused<<<256, 256, 0, stream>>>(x, ws, b1, b2, out, 0);
    k_col<<<256, 256, 0, stream>>>(ws, nullptr, nullptr KID(3));
    k_fused<<<256, 256, 0, stream>>>(x2, ws, b1, b2, out, 1);
}
```
